```python
import torch

def _softmax(x, axis=-1):
    return torch.softmax(x, dim=axis)
def _gelu(x):
    return torch.nn.functional.gelu(x, approximate="tanh")
def _silu(x):
    return torch.nn.functional.silu(x)
def _sigmoid(x):
    return torch.sigmoid(x)
def _layernorm(x, axis=-1, eps=1e-5):
    m = x.mean(dim=axis, keepdim=True)
    v = x.var(dim=axis, keepdim=True, unbiased=False)
    return (x - m) / torch.sqrt(v + eps)
def _scatter_add(tgt, idx, src, axis=0):
    return tgt.clone().index_add_(axis, idx, src)
def _segment_sum(src, seg, n):
    out = torch.zeros((n,) + src.shape[1:], dtype=src.dtype)
    return out.index_add_(0, seg, src)
def _pad(x, pw):
    flat = tuple(v for pair in reversed(pw) for v in pair)
    return torch.nn.functional.pad(x, flat)

kernel_name = 'standalone__edge_featured_mpnn_gatedgcn__graph'

def setup_inputs():

    _w = (lambda a: (lambda t: t.to(torch.bfloat16) if t.is_floating_point() else t)(torch.as_tensor(a)))
    N, deg, D, De = 25000, 16, 128, max(4, 1)
    E = N * deg
    return {
        "x": _w((lambda *s: torch.randn(*s, dtype=torch.bfloat16) * 0.4)(N, D)),
        "e_attr": _w((lambda *s: torch.randn(*s, dtype=torch.bfloat16) * 0.8)(E, De)),
        "src": _w((lambda lo, hi, *s: torch.randint(lo, hi, s))(0, N, E).to(torch.int64)),
        "dst": _w((lambda lo, hi, *s: torch.randint(lo, hi, s))(0, N, E).to(torch.int64)),
        "w_a": _w((lambda *s: torch.randn(*s, dtype=torch.bfloat16) * 0.4)(D, D)),
        "w_b": _w((lambda *s: torch.randn(*s, dtype=torch.bfloat16) * 0.4)(D, D)),
        "w_c": _w((lambda *s: torch.randn(*s, dtype=torch.bfloat16) * 0.8)(De, D)),
        "w_u": _w((lambda *s: torch.randn(*s, dtype=torch.bfloat16) * 0.05)(D, D)),
        "w_v": _w((lambda *s: torch.randn(*s, dtype=torch.bfloat16) * 0.05)(D, D)),
    }

def reference(x, e_attr, src, dst, w_a, w_b, w_c, w_u, w_v):
    N, D = x.shape
    hv = x @ w_v
    g = x[dst] @ w_a + x[src] @ w_b + e_attr @ w_c
    eta = _sigmoid(g)
    denom = _segment_sum(eta, dst, N) + 1e-6
    eta_n = eta / denom[dst]
    agg = _segment_sum(eta_n * hv[src], dst, N)
    z = x @ w_u + agg
    relu_z = torch.where(z > 0, z, 0 * z)
    return x + relu_z
```

```cpp
#include <hip/hip_runtime.h>
#include <hip/hip_bf16.h>
#include <stdint.h>

typedef unsigned u32;
typedef unsigned short u16;
typedef __attribute__((ext_vector_type(8))) __bf16 bf16x8;
typedef __attribute__((ext_vector_type(2))) __bf16 bf16x2;
typedef __attribute__((ext_vector_type(2))) float f32x2;
typedef __attribute__((ext_vector_type(16))) float f32x16;
typedef __attribute__((ext_vector_type(4))) u32 u32x4;
typedef __attribute__((ext_vector_type(2))) u32 u32x2;
typedef __attribute__((ext_vector_type(3))) u32 u32x3;
typedef __attribute__((ext_vector_type(4))) short s16x4;
#define DI __device__ __forceinline__
#define LDS_PTR(T) __attribute__((address_space(3))) T*

constexpr int N_NODES = 25000;
constexpr int N_EDGES = 400000;
constexpr int DIM = 128;
constexpr int NPB = 25;
constexpr int NBUCK = N_NODES / NPB;
constexpr int NCHUNK = 60;
constexpr int CH = (N_EDGES + NCHUNK - 1) / NCHUNK;
constexpr int K1_THREADS = 1024;
constexpr int EPT = (CH + K1_THREADS - 1) / K1_THREADS;
constexpr int GEMM_BLOCKS = (N_NODES + 127) / 128;
constexpr int WIN = 480;
constexpr int WROUNDS = (WIN + 63) / 64;
constexpr float NLOG2E = -1.44269504088896340736f;

constexpr size_t WS_TABLE = 0;
constexpr size_t WS_RECS = WS_TABLE + (size_t)4 * N_NODES * 128;
constexpr size_t WS_OFFS = WS_RECS + (((size_t)NCHUNK * CH * 12 + 255) / 256) * 256;

DI f32x16 mfma32(u32x4 a, u32x4 b, f32x16 c) {
  return __builtin_amdgcn_mfma_f32_32x32x16_bf16(__builtin_bit_cast(bf16x8, a), __builtin_bit_cast(bf16x8, b), c, 0, 0, 0);
}
DI u32 cvtpk(float lo, float hi) {
  f32x2 v = {lo, hi};
  bf16x2 b = __builtin_convertvector(v, bf16x2);
  return __builtin_bit_cast(u32, b);
}
DI u32x2 tr_read(u32 lds_addr) {
  s16x4 r = __builtin_amdgcn_ds_read_tr16_b64_v4i16((LDS_PTR(s16x4))(size_t)lds_addr);
  return __builtin_bit_cast(u32x2, r);
}
template <class T> DI T lds_ld(u32 a) { return *(LDS_PTR(T))(size_t)a; }
template <class T> DI void lds_st(u32 a, T v) { *(LDS_PTR(T))(size_t)a = v; }
DI u32 lds_addr_of(const void* p) { return (u32)(size_t)(LDS_PTR(const char))p; }

DI u32 wave_incl_scan(u32 v) {
  v += __builtin_amdgcn_update_dpp(0u, v, 0x111, 0xf, 0xf, false);
  v += __builtin_amdgcn_update_dpp(0u, v, 0x112, 0xf, 0xf, false);
  v += __builtin_amdgcn_update_dpp(0u, v, 0x114, 0xf, 0xf, false);
  v += __builtin_amdgcn_update_dpp(0u, v, 0x118, 0xf, 0xf, false);
  v += __builtin_amdgcn_update_dpp(0u, v, 0x142, 0xa, 0xf, false);
  v += __builtin_amdgcn_update_dpp(0u, v, 0x143, 0xc, 0xf, false);
  return v;
}

__global__ __launch_bounds__(K1_THREADS) void gg_prep_kernel(
    const u16* __restrict__ x, const u32* __restrict__ eattr, const int* __restrict__ src, const int* __restrict__ dst,
    const u16* __restrict__ w_b, const u16* __restrict__ w_v, char* __restrict__ ws) {
  __shared__ __attribute__((aligned(16))) char smem[65536];
  const int tid = threadIdx.x;
  const int lane = tid & 63, wave = tid >> 6;

  if (blockIdx.x < NCHUNK) {
    const int c = blockIdx.x;
    u32* hist = (u32*)smem;
    u32* wsum = hist + 1024;
    const int e0 = c * CH;
    const int e1 = min(e0 + CH, N_EDGES);
    int dv[EPT], sv[EPT];
    u32x2 ev[EPT];
#pragma unroll
    for (int k = 0; k < EPT; ++k) {
      int e = e0 + tid + k * K1_THREADS;
      bool ok = e < e1;
      int ec = ok ? e : e0;
      dv[k] = ok ? dst[ec] : -1;
      sv[k] = src[ec];
      ev[k] = *(const u32x2*)(eattr + 2 * (size_t)ec);
    }
    hist[tid] = 0;
    __syncthreads();
    u32 rank[EPT], bk[EPT];
#pragma unroll
    for (int k = 0; k < EPT; ++k) {
      u32 b = (u32)dv[k] / NPB;
      bk[k] = b;
      rank[k] = 0;
      if (dv[k] >= 0) rank[k] = atomicAdd(&hist[b], 1u);
    }
    __syncthreads();
    u32 cnt = hist[tid];
    u32 inc = wave_incl_scan(cnt);
    if (lane == 63) wsum[wave] = inc;
    __syncthreads();
    u32 base = 0;
    {
      u32 ws_ = (lane < 16) ? wsum[lane] : 0u;
      u32 wi = wave_incl_scan(ws_);
      base = __builtin_amdgcn_readlane(wi - ws_, wave & 15) ;
    }
    u32 start = base + inc - cnt;
    hist[tid] = start;
    u32* offs = (u32*)(ws + WS_OFFS);
    if (tid < NBUCK) offs[tid * 64 + c] = start | (cnt << 16);
    __syncthreads();
    char* recs = ws + WS_RECS + (size_t)c * CH * 12;
#pragma unroll
    for (int k = 0; k < EPT; ++k) {
      if (dv[k] >= 0) {
        u32 pos = hist[bk[k]] + rank[k];
        u32 dl = (u32)dv[k] - bk[k] * NPB;
        u32x3 r = {(u32)sv[k] * 128u + dl + 1u, ev[k][0], ev[k][1]};
        *(u32x3*)(recs + (size_t)pos * 12) = r;
      }
    }
  } else {
    const int gb = blockIdx.x - NCHUNK;
    const int g = wave & 3, tile = wave >> 2;
    const int m = lane & 31, h = lane >> 5;
    const int n0 = gb * 128 + tile * 32;
    u32x4 xf[8];
    {
      int node = min(n0 + m, N_NODES - 1);
      const char* xp = (const char*)x + (size_t)node * 256 + h * 16;
#pragma unroll
      for (int s = 0; s < 8; ++s) xf[s] = *(const u32x4*)(xp + s * 32);
    }
    const u32 sbase = lds_addr_of(smem);
#pragma unroll
    for (int i = 0; i < 4; ++i) {
      int q = tid + K1_THREADS * i;
      int mat = q >> 11, k = (q >> 4) & 127, c16 = q & 15;
      const u16* w = mat ? w_v : w_b;
      u32x4 v = *(const u32x4*)((const char*)w + k * 256 + c16 * 16);
      lds_st<u32x4>(sbase + mat * 32768 + (c16 >> 2) * 8192 + k * 64 + (c16 & 3) * 16, v);
    }
    __syncthreads();
    const int G = lane >> 4, q4 = (lane & 15) >> 2, p4 = lane & 3;
    const u32 wa = sbase + g * 8192 + (8 * h + q4) * 64 + (16 * (G & 1) + 4 * p4) * 2;
    f32x16 ab, av;
#pragma unroll
    for (int i = 0; i < 16; ++i) { ab[i] = 0.f; av[i] = 0.f; }
#pragma unroll
    for (int s = 0; s < 8; ++s) {
      u32x2 b0 = tr_read(wa + s * 1024), b1 = tr_read(wa + s * 1024 + 256);
      u32x2 v0 = tr_read(wa + 32768 + s * 1024), v1 = tr_read(wa + 32768 + s * 1024 + 256);
      u32x4 fb = {b0[0], b0[1], b1[0], b1[1]};
      u32x4 fv = {v0[0], v0[1], v1[0], v1[1]};
      ab = mfma32(xf[s], fb, ab);
      av = mfma32(xf[s], fv, av);
    }
    u32* T = (u32*)(ws + WS_TABLE) + (size_t)g * N_NODES * 32;
#pragma unroll
    for (int i = 0; i < 16; ++i) {
      int node = n0 + (i & 3) + 8 * (i >> 2) + 4 * h;
      u32 lo = cvtpk(av[i], 0.f) & 0xffffu;
      u32 bits = __builtin_bit_cast(u32, ab[i] * NLOG2E);
      u32 mag = bits & 0x7fffffffu;
      u32 t = mag + 0x8000u;
      t = (t > lo ? t : lo) - lo;
      u32 wv = (t & 0x7fff0000u) | (bits & 0x80000000u) | lo;
      if (node < N_NODES) T[(size_t)node * 32 + m] = wv;
    }
  }
}

constexpr int L_SO = 0;
constexpr int L_DS = L_SO + 4 * WIN * 4;
constexpr int L_DA = L_DS + 4 * WIN * 4;
constexpr int L_E = L_DA + 4 * WIN * 2;
constexpr int L_LA = L_E + 4 * 2 * WIN * 4;
constexpr int L_LS = L_LA + 2048;
constexpr int L_TOTAL = L_LS + 4096;
static_assert(L_TOTAL <= 40960, "LDS budget");
static_assert(L_E >= 16384, "weight image aliasing");

__global__ __launch_bounds__(256, 4) void gg_agg_kernel(
    const u16* __restrict__ x, const u16* __restrict__ w_a, const u16* __restrict__ w_c, const u16* __restrict__ w_u,
    const char* __restrict__ ws, u16* __restrict__ out) {
  __shared__ __attribute__((aligned(16))) char smem[L_TOTAL];
  const int tid = threadIdx.x;
  const int lane = tid & 63;
  const int w = __builtin_amdgcn_readfirstlane(tid >> 6);
  const int b = blockIdx.x;
  const int g = (b & 7) >> 1;
  const int fb = (((b & 1) * 125 + (b >> 3)) << 2) + w;
  const int m = lane & 31, h = lane >> 5;
  const int G = lane >> 4, q4 = (lane & 15) >> 2, p4 = lane & 3;
  const u32 sb = lds_addr_of(smem);

  const u32* offs = (const u32*)(ws + WS_OFFS);
  u32 oe = offs[fb * 64 + min(lane, NCHUNK - 1)];
  oe = (lane < NCHUNK) ? oe : 0u;
  u32x4 xf[8];
  {
    const char* xp = (const char*)x + (size_t)(fb * NPB) * 256;
    const u32 xo = min(m, NPB - 1) * 256 + h * 16;
#pragma unroll
    for (int s = 0; s < 8; ++s) xf[s] = *(const u32x4*)(xp + (size_t)(xo + s * 32));
  }
#pragma unroll
  for (int i = 0; i < 4; ++i) {
    int q = tid + 256 * i;
    int mat = q >> 9, k = (q >> 2) & 127, c4 = q & 3;
    const u16* wp = mat ? w_u : w_a;
    u32x4 v = *(const u32x4*)((const char*)wp + k * 256 + g * 64 + c4 * 16);
    lds_st<u32x4>(sb + mat * 8192 + k * 64 + c4 * 16, v);
  }
  if (tid < 128) {
    int hh = tid >> 6, ss = (tid >> 5) & 1, dl = tid & 31;
    u32x4 v;
#pragma unroll
    for (int r = 0; r < 4; ++r) {
      int row0 = 16 * ss + 8 * (r >> 1) + 4 * hh + 2 * (r & 1);
      u32 d = 0;
      if (dl >= 1 && dl <= NPB) d = (dl - 1 == row0) ? 0x3f80u : ((dl - 1 == row0 + 1) ? 0x3f800000u : 0u);
      v[r] = d;
    }
    lds_st<u32x4>(sb + L_LA + tid * 16, v);
  }
  {
    int dl = tid >> 3, part = tid & 7;
    int col0 = (part & 3) * 8;
    u32x4 v;
#pragma unroll
    for (int r = 0; r < 4; ++r) {
      int c0 = col0 + 2 * r;
      u32 d = 0;
      if (dl >= 1 && dl <= NPB) d = (dl - 1 == c0) ? 0x3f80u : ((dl - 1 == c0 + 1) ? 0x3f800000u : 0u);
      v[r] = d;
    }
    lds_st<u32x4>(sb + L_LS + tid * 16, v);
  }
  u32 wcpair;
  {
    int r0 = h ? 0 : 2;
    float a = __builtin_bit_cast(float, (u32)w_c[r0 * DIM + g * 32 + m] << 16) * NLOG2E;
    float c = __builtin_bit_cast(float, (u32)w_c[(r0 + 1) * DIM + g * 32 + m] << 16) * NLOG2E;
    wcpair = cvtpk(a, c);
  }

  const u32 cnt = oe >> 16;
  const u32 inc = wave_incl_scan(cnt);
  const u32 pre = inc - cnt;
  const int M = __builtin_amdgcn_readlane(inc, 63);
  const u32 rbase = (lane * CH + (oe & 0xffffu)) * 12u;
  const __amdgpu_buffer_rsrc_t rrs = __builtin_amdgcn_make_buffer_rsrc((void*)(ws + WS_RECS), 0, NCHUNK * CH * 12, 0x00020000);

  const u32 so_w = sb + L_SO + w * (WIN * 4);
  const u32 ds_w = sb + L_DS + w * (WIN * 4);
  const u32 da_w = sb + L_DA + w * (WIN * 2);
  const u32 e_w = sb + L_E + w * (WIN * 8);

  u32x3 rec[WROUNDS];
  auto expand_and_load = [&](int w0) {
    int ln = lane;
    asm volatile("" : "+v"(ln));
    int jlo = max(0, w0 - (int)pre);
    int jhi = min((int)cnt, w0 + WIN - (int)pre);
    {
      u32 la = e_w + ((int)pre + jlo - w0) * 4;
      u32 rv = rbase + jlo * 12;
      for (int j = jlo; j < jhi; ++j) { lds_st<u32>(la, rv); la += 4; rv += 12; }
    }
    int mw = min(WIN, M - w0);
    const u32 el = e_w + ln * 4;
#pragma unroll
    for (int k = 0; k < WROUNDS; ++k) {
      u32 a = lds_ld<u32>(el + 256 * k);
      a = (ln < mw - 64 * k) ? a : 0x7ffffff0u;
      rec[k] = __builtin_bit_cast(u32x3, __builtin_amdgcn_raw_buffer_load_b96(rrs, a, 0, 0));
    }
  };
  auto write_lists = [&]() {
    int ln = lane;
    asm volatile("" : "+v"(ln));
    const u32 so_a = so_w + ln * 4;
    const u32 da_a = da_w + ln * 2;
    const u32 ds_a = ds_w + ((ln & 32) + ((ln & 7) << 2) + ((ln >> 3) & 3)) * 4;
    const u32 e_a = e_w + ln * 4;
#pragma unroll
    for (int k = 0; k < WROUNDS; ++k) {
      if (k * 64 + 63 < WIN || ln < WIN - 64 * k) {
        u32 r0 = rec[k][0];
        u32 dl = r0 & 31u;
        lds_st<u32>(so_a + 256 * k, r0 & ~127u);
        lds_st<u16>(da_a + 128 * k, (u16)(dl * 16));
        lds_st<u32>(ds_a + 256 * k, dl * 128);
        lds_st<u32>(e_a + 256 * k, rec[k][2]);
        lds_st<u32>(e_a + WIN * 4 + 256 * k, rec[k][1]);
      }
    }
  };

  expand_and_load(0);
  __syncthreads();

  u32x4 nb0, nb1;
  u32 upk[8];
  {
    const u32 wa = sb + (8 * h + q4) * 64 + (16 * (G & 1) + 4 * p4) * 2;
    f32x16 aa, au;
#pragma unroll
    for (int i = 0; i < 16; ++i) { aa[i] = 0.f; au[i] = 0.f; }
#pragma unroll
    for (int s = 0; s < 8; ++s) {
      u32x2 a0 = tr_read(wa + s * 1024), a1 = tr_read(wa + s * 1024 + 256);
      u32x2 u0 = tr_read(wa + 8192 + s * 1024), u1 = tr_read(wa + 8192 + s * 1024 + 256);
      u32x4 fa = {a0[0], a0[1], a1[0], a1[1]};
      u32x4 fu = {u0[0], u0[1], u1[0], u1[1]};
      aa = mfma32(xf[s], fa, aa);
      au = mfma32(xf[s], fu, au);
    }
#pragma unroll
    for (int r = 0; r < 4; ++r) {
      nb0[r] = cvtpk(aa[2 * r] * NLOG2E, aa[2 * r + 1] * NLOG2E);
      nb1[r] = cvtpk(aa[8 + 2 * r] * NLOG2E, aa[8 + 2 * r + 1] * NLOG2E);
    }
    nb1[3] = wcpair;
#pragma unroll
    for (int r = 0; r < 8; ++r) { upk[r] = cvtpk(au[2 * r], au[2 * r + 1]); asm volatile("" : "+v"(upk[r])); }
#pragma unroll
    for (int r = 0; r < 4; ++r) { asm volatile("" : "+v"(nb0[r])); asm volatile("" : "+v"(nb1[r])); }
  }
  __syncthreads();

  f32x16 num, den;
#pragma unroll
  for (int i = 0; i < 16; ++i) { num[i] = 0.f; den[i] = 0.f; }

  const char* tb = ws + WS_TABLE + (size_t)g * N_NODES * 128;
  const u32 m4 = m * 4;
  const u32 so_l = so_w + h * 16;
  const u32 ds_l = ds_w + (h * 4 + q4) * 16;
  const u32 da_l = da_w + m * 2;
  const u32 e_l = e_w + h * (WIN * 4) + m * 4;
  const u32 la_l = sb + L_LA + h * 1024;
  const u32 ls_l = sb + L_LS + (q4 & 1) * 64 + (16 * (G & 1) + 4 * p4) * 2;

  for (int w0 = 0;;) {
    write_lists();
    const int mw = min(WIN, M - w0);
    const int nt = (mw + 31) >> 5;
    for (int t = 0; t < nt; ++t) {
      u32 raw[16];
#pragma unroll
      for (int q = 0; q < 4; ++q) {
        u32x4 o = lds_ld<u32x4>(so_l + t * 128 + q * 32);
#pragma unroll
        for (int k = 0; k < 4; ++k) raw[q * 4 + k] = *(const u32*)(tb + (size_t)(u32)(o[k] + m4));
      }
      u32 da = lds_ld<u16>(da_l + t * 64);
      u32 la = la_l + da;
      u32x4 a0 = lds_ld<u32x4>(la);
      u32x2 a1lo = lds_ld<u32x2>(la + 512);
      u32 a1z = lds_ld<u32>(la + 512 + 8);
      u32 ee = lds_ld<u32>(e_l + t * 128);
      u32x4 a1 = {a1lo[0], a1lo[1], a1z, ee};
      u32x4 dsv = lds_ld<u32x4>(ds_l + t * 128);
      u32x2 s00 = tr_read(ls_l + dsv[0]), s01 = tr_read(ls_l + dsv[1]);
      u32x2 s10 = tr_read(ls_l + dsv[2]), s11 = tr_read(ls_l + dsv[3]);
      u32x4 S0 = {s00[0], s00[1], s01[0], s01[1]};
      u32x4 S1 = {s10[0], s10[1], s11[0], s11[1]};

      f32x16 gt;
#pragma unroll
      for (int i = 0; i < 16; ++i) gt[i] = __builtin_bit_cast(float, raw[i]);
      gt = mfma32(a0, nb0, gt);
      gt = mfma32(a1, nb1, gt);
      float eta[16], msg[16];
#pragma unroll
      for (int i = 0; i < 16; ++i) {
        float e = __builtin_amdgcn_exp2f(gt[i]);
        eta[i] = __builtin_amdgcn_rcpf(1.0f + e);
        msg[i] = eta[i] * __builtin_bit_cast(float, raw[i] << 16);
      }
      u32x4 P0, P1, Q0, Q1;
#pragma unroll
      for (int r = 0; r < 4; ++r) {
        P0[r] = cvtpk(eta[2 * r], eta[2 * r + 1]);
        P1[r] = cvtpk(eta[8 + 2 * r], eta[8 + 2 * r + 1]);
        Q0[r] = cvtpk(msg[2 * r], msg[2 * r + 1]);
        Q1[r] = cvtpk(msg[8 + 2 * r], msg[8 + 2 * r + 1]);
      }
      den = mfma32(S0, P0, den);
      num = mfma32(S0, Q0, num);
      den = mfma32(S1, P1, den);
      num = mfma32(S1, Q1, num);
    }
    w0 += WIN;
    if (w0 >= M) break;
    expand_and_load(w0);
  }

  const int n0 = fb * NPB;
#pragma unroll
  for (int i = 0; i < 13; ++i) {
    int row = (i & 3) + 8 * (i >> 2) + 4 * h;
    if (i < 12 || h == 0) {
      size_t o = (size_t)(n0 + row) * DIM + g * 32 + m;
      float xv = __builtin_bit_cast(float, (u32)x[o] << 16);
      u32 up = upk[i >> 1];
      float u = __builtin_bit_cast(float, (i & 1) ? (up & 0xffff0000u) : (up << 16));
      float z = u + num[i] * __builtin_amdgcn_rcpf(den[i] + 1e-6f);
      float r = xv + fmaxf(z, 0.f);
      out[o] = (u16)(cvtpk(r, 0.f) & 0xffffu);
    }
  }
}

extern "C" void kernel_launch(void* const* d_in, const int* in_sizes, int n_in,
                              void* d_out, int out_size, void* d_ws, size_t ws_size,
                              hipStream_t stream) {
  const u16* x = (const u16*)d_in[0];
  const u32* eattr = (const u32*)d_in[1];
  const int* src = (const int*)d_in[2];
  const int* dst = (const int*)d_in[3];
  const u16* w_a = (const u16*)d_in[4];
  const u16* w_b = (const u16*)d_in[5];
  const u16* w_c = (const u16*)d_in[6];
  const u16* w_u = (const u16*)d_in[7];
  const u16* w_v = (const u16*)d_in[8];
  char* ws = (char*)d_ws;
  gg_prep_kernel<<<NCHUNK + GEMM_BLOCKS, K1_THREADS, 0, stream>>>(x, eattr, src, dst, w_b, w_v, ws);
  gg_agg_kernel<<<NBUCK, 256, 0, stream>>>(x, w_a, w_c, w_u, ws, (u16*)d_out);
}
```

```cpp
#include <hip/hip_runtime.h>
#include <hip/hip_bf16.h>
#include <stdint.h>

typedef unsigned u32;
typedef unsigned short u16;
typedef __attribute__((ext_vector_type(8))) __bf16 bf16x8;
typedef __attribute__((ext_vector_type(2))) __bf16 bf16x2;
typedef __attribute__((ext_vector_type(2))) float f32x2;
typedef __attribute__((ext_vector_type(4))) float f32x4;
typedef __attribute__((ext_vector_type(16))) float f32x16;
typedef __attribute__((ext_vector_type(4))) u32 u32x4;
typedef __attribute__((ext_vector_type(8))) u32 u32x8;
typedef __attribute__((ext_vector_type(16))) u32 u32x16;
typedef __attribute__((ext_vector_type(2))) u32 u32x2;
typedef __attribute__((ext_vector_type(3))) u32 u32x3;
typedef __attribute__((ext_vector_type(4))) short s16x4;
#define DI __device__ __forceinline__
#define LDS_PTR(T) __attribute__((address_space(3))) T*

constexpr int N_NODES = 25000;
constexpr int N_EDGES = 400000;
constexpr int DIM = 128;
constexpr int NPB = 25;
constexpr int NBUCK = N_NODES / NPB;
constexpr int NCHUNK = 56;
constexpr int CH = 7168;
constexpr int K1_THREADS = 1024;
constexpr int BPB = 5;
constexpr int GEMM_BLOCKS = NBUCK / BPB;
constexpr int WIN = 512;
constexpr float NLOG2E = -1.44269504088896340736f;
static_assert(NCHUNK * CH >= N_EDGES && N_EDGES % 8 == 0, "chunking");

constexpr size_t WS_TABLE = 256;
constexpr size_t WS_AU = WS_TABLE + (size_t)4 * N_NODES * 128;
constexpr int AU_UNIT = 3328;
constexpr size_t WS_RECS = WS_AU + (size_t)4 * NBUCK * AU_UNIT;
constexpr size_t WS_OFFS = WS_RECS + (size_t)NCHUNK * CH * 12;
constexpr size_t WS_END = WS_OFFS + (size_t)NCHUNK * 1024 * 4;

DI f32x16 mfma32(u32x4 a, u32x4 b, f32x16 c) {
  return __builtin_amdgcn_mfma_f32_32x32x16_bf16(__builtin_bit_cast(bf16x8, a), __builtin_bit_cast(bf16x8, b), c, 0, 0, 0);
}
DI u32 cvtpk(float lo, float hi) {
  f32x2 v = {lo, hi};
  bf16x2 b = __builtin_convertvector(v, bf16x2);
  return __builtin_bit_cast(u32, b);
}
DI u32x2 tr_read(u32 lds_addr) {
  s16x4 r = __builtin_amdgcn_ds_read_tr16_b64_v4i16((LDS_PTR(s16x4))(size_t)lds_addr);
  return __builtin_bit_cast(u32x2, r);
}
template <class T> DI T lds_ld(u32 a) { return *(LDS_PTR(T))(size_t)a; }
template <class T> DI void lds_st(u32 a, T v) { *(LDS_PTR(T))(size_t)a = v; }
DI u32 lds_addr_of(const void* p) { return (u32)(size_t)(LDS_PTR(const char))p; }
DI void glds16(const void* g, u32 lds_uniform) {
  __builtin_amdgcn_global_load_lds((const __attribute__((address_space(1))) void*)g, (LDS_PTR(void))(size_t)lds_uniform, 16, 0, 0);
}

constexpr int STAUX = 16, OUTAUX = 16;
typedef __amdgpu_buffer_rsrc_t rsrc_t;
DI rsrc_t mk_rsrc(const void* p, u32 bytes) { return __builtin_amdgcn_make_buffer_rsrc((void*)p, 0, bytes, 0x00020000); }
template <int AUX> DI void bst(rsrc_t r, u32 o, u32 v) { __builtin_amdgcn_raw_buffer_store_b32(v, r, o, 0, AUX); }
template <int AUX> DI void bst(rsrc_t r, u32 o, u32x2 v) { __builtin_amdgcn_raw_buffer_store_b64(v, r, o, 0, AUX); }
template <int AUX> DI void bst(rsrc_t r, u32 o, u32x3 v) { __builtin_amdgcn_raw_buffer_store_b96(v, r, o, 0, AUX); }
template <int AUX> DI void bst(rsrc_t r, u32 o, u32x4 v) { __builtin_amdgcn_raw_buffer_store_b128(v, r, o, 0, AUX); }
DI u32 wave_incl_scan(u32 v) {
  v += __builtin_amdgcn_update_dpp(0u, v, 0x111, 0xf, 0xf, false);
  v += __builtin_amdgcn_update_dpp(0u, v, 0x112, 0xf, 0xf, false);
  v += __builtin_amdgcn_update_dpp(0u, v, 0x114, 0xf, 0xf, false);
  v += __builtin_amdgcn_update_dpp(0u, v, 0x118, 0xf, 0xf, false);
  v += __builtin_amdgcn_update_dpp(0u, v, 0x142, 0xa, 0xf, false);
  v += __builtin_amdgcn_update_dpp(0u, v, 0x143, 0xc, 0xf, false);
  return v;
}

constexpr int K1_LDS = 163840;
__global__ __launch_bounds__(K1_THREADS) void gg_prep_kernel(
    const u16* __restrict__ x, const u32* __restrict__ eattr, const int* __restrict__ src, const int* __restrict__ dst,
    const u16* __restrict__ w_a, const u16* __restrict__ w_b, const u16* __restrict__ w_u, const u16* __restrict__ w_v,
    char* __restrict__ ws) {
  __shared__ __attribute__((aligned(16))) char smem[K1_LDS];
  const int tid = threadIdx.x;
  const int lane = tid & 63;
  const int wave = __builtin_amdgcn_readfirstlane(tid >> 6);
  const u32 sb = lds_addr_of(smem);

  if (blockIdx.x < NCHUNK) {
    const int c = blockIdx.x;
    const u32 hist = sb;
    const u32 wsum = sb + 4096;
    const u32 stage = sb + 4352;
    const int e0 = c * CH + tid * 8;
    const bool act = (tid < CH / 8) && (e0 < N_EDGES);
    const int eb = act ? e0 : 0;
    u32x4 dv[2], sv[2], ev[4];
    if (wave < CH / 8 / 64) {
#pragma unroll
      for (int k = 0; k < 2; ++k) dv[k] = *(const u32x4*)(dst + eb + 4 * k);
#pragma unroll
      for (int k = 0; k < 2; ++k) sv[k] = *(const u32x4*)(src + eb + 4 * k);
#pragma unroll
      for (int k = 0; k < 4; ++k) ev[k] = *(const u32x4*)(eattr + 2 * (size_t)eb + 4 * k);
    } else {
#pragma unroll
      for (int k = 0; k < 2; ++k) { dv[k] = u32x4{0u, 0u, 0u, 0u}; sv[k] = dv[k]; }
#pragma unroll
      for (int k = 0; k < 4; ++k) ev[k] = dv[0];
    }
    lds_st<u32>(hist + tid * 4, 0u);
    __syncthreads();
    u32 rank[8], ha[8], key[8];
    if (act) {
      u32 bk[8];
#pragma unroll
      for (int k = 0; k < 8; ++k) {
        bk[k] = __umul24(dv[k >> 2][k & 3], 5243u) >> 17;
        ha[k] = hist + bk[k] * 4;
        rank[k] = __hip_atomic_fetch_add((LDS_PTR(u32))(size_t)ha[k], 1u, __ATOMIC_RELAXED, __HIP_MEMORY_SCOPE_WORKGROUP);
      }
      __builtin_amdgcn_sched_barrier(0);
#pragma unroll
      for (int k = 0; k < 8; ++k) {
        u32 dl;
        asm("v_mad_i32_i24 %0, %1, %2, %3" : "=v"(dl) : "v"(bk[k]), "s"(-NPB), "v"(dv[k >> 2][k & 3]));
        key[k] = sv[k >> 2][k & 3] * 128u + dl + 1u;
      }
    } else {
#pragma unroll
      for (int k = 0; k < 8; ++k) { ha[k] = hist; rank[k] = 0; key[k] = 0; }
    }
    __syncthreads();
    const u32 cnt = lds_ld<u32>(hist + tid * 4);
    const u32 inc = wave_incl_scan(cnt);
    if (lane == 63) lds_st<u32>(wsum + wave * 4, inc);
    __syncthreads();
    u32 base, total;
    {
      u32 ws_ = lds_ld<u32>(wsum + (lane & 15) * 4);
      ws_ = (lane < 16) ? ws_ : 0u;
      u32 wi = wave_incl_scan(ws_);
      base = __builtin_amdgcn_readlane(wi - ws_, wave);
      total = __builtin_amdgcn_readlane(wi, 15);
    }
    const u32 start = base + inc - cnt;
    lds_st<u32>(hist + tid * 4, stage + __umul24(start, 12u));
    const rsrc_t wsr = mk_rsrc(ws, (u32)WS_END);
    bst<STAUX>(wsr, (u32)WS_OFFS + (c * 1024 + tid) * 4, start | (cnt << 16));
    __syncthreads();
    if (act) {
      u32 a[8];
#pragma unroll
      for (int k = 0; k < 8; ++k) a[k] = lds_ld<u32>(ha[k]);
#pragma unroll
      for (int k = 0; k < 8; ++k) {
        a[k] += __umul24(rank[k], 12u);
        lds_st<u32>(a[k], key[k]);
        lds_st<u32>(a[k] + 4, ev[k >> 1][(k & 1) * 2]);
        lds_st<u32>(a[k] + 8, ev[k >> 1][(k & 1) * 2 + 1]);
      }
    }
    __syncthreads();
    const rsrc_t rcr = mk_rsrc(ws + WS_RECS + (size_t)c * (CH * 12), (total * 12 + 15) & ~15u);
    constexpr int NCP = CH * 12 / 16 / K1_THREADS;
    u32x4 cp[NCP + 1];
#pragma unroll
    for (int k = 0; k < NCP; ++k) cp[k] = lds_ld<u32x4>(stage + (tid + k * K1_THREADS) * 16);
    if (wave < (CH * 12 / 16 - NCP * K1_THREADS) / 64) cp[NCP] = lds_ld<u32x4>(stage + (tid + NCP * K1_THREADS) * 16);
#pragma unroll
    for (int k = 0; k < NCP; ++k) bst<STAUX>(rcr, (tid + k * K1_THREADS) * 16, cp[k]);
    if (wave < (CH * 12 / 16 - NCP * K1_THREADS) / 64) bst<STAUX>(rcr, (tid + NCP * K1_THREADS) * 16, cp[NCP]);
  } else {
    const int j = blockIdx.x - NCHUNK;
    const int n0 = j * (BPB * NPB);
    const u32 XI = sb, WI = sb + 32768;
#pragma unroll
    for (int q = 0; q < 2; ++q) {
      int i = wave + 16 * q;
      int row = 4 * i + (lane >> 4);
      int node = min(n0 + row, N_NODES - 1);
      glds16((const char*)x + (size_t)node * 256 + (((lane & 15) ^ (row & 15)) << 4), XI + i * 1024);
    }
#pragma unroll
    for (int q = 0; q < 8; ++q) {
      int i = wave + 16 * q;
      int mat = q >> 1;
      int k = 4 * (i & 31) + (lane >> 4);
      const u16* wm = mat == 0 ? w_b : mat == 1 ? w_v : mat == 2 ? w_a : w_u;
      glds16((const char*)wm + k * 256 + (((lane & 15) ^ ((k & 3) << 2)) << 4), WI + i * 1024);
    }
    const int g = wave & 3, role = wave >> 2;
    const int m = lane & 31, h = lane >> 5;
    const int G = lane >> 4, q4 = (lane & 15) >> 2, p4 = lane & 3;
    __syncthreads();
    const rsrc_t wsr = mk_rsrc(ws, (u32)WS_END);
    const rsrc_t tbr = mk_rsrc(ws + WS_TABLE + (size_t)g * N_NODES * 128, N_NODES * 128);
    float tiny = 0x1p-90f;
    asm volatile("" : "+v"(tiny));
    u32x4 w0[8], w1[8];
    {
      const u32 wa = WI + (role >> 1) * 65536 + (8 * h + q4) * 256 + (((4 * g + 2 * (G & 1) + (p4 >> 1)) ^ (q4 << 2)) << 4) + (p4 & 1) * 8;
#pragma unroll
      for (int s = 0; s < 8; ++s) {
        u32x2 a0 = tr_read(wa + s * 4096), a1 = tr_read(wa + s * 4096 + 1024);
        u32x2 b0 = tr_read(wa + 32768 + s * 4096), b1 = tr_read(wa + 32768 + s * 4096 + 1024);
        w0[s] = u32x4{a0[0], a0[1], a1[0], a1[1]};
        w1[s] = u32x4{b0[0], b0[1], b1[0], b1[1]};
      }
    }
    const int t0 = (role == 0) ? 0 : (role == 1) ? 2 : (role == 2) ? 0 : 3;
    const int t1 = (role == 0) ? 2 : (role == 1) ? 4 : (role == 2) ? 3 : 5;
    const int rstep = (role < 2) ? 32 : NPB;
    for (int t = t0; t < t1; ++t) {
      const int r = min(t * rstep + m, 127);
      const u32 xa = XI + r * 256 + (((r & 15) ^ h) << 4);
      f32x16 c0, c1;
#pragma unroll
      for (int i = 0; i < 16; ++i) { c0[i] = 0.f; c1[i] = 0.f; }
#pragma unroll
      for (int s = 0; s < 8; ++s) {
        u32x4 xf = lds_ld<u32x4>(xa ^ (s << 5));
        c0 = mfma32(xf, w0[s], c0);
        c1 = mfma32(xf, w1[s], c1);
      }
      if (role < 2) {
        const u32 T = (n0 + t * 32 + 4 * h) * 128 + m * 4;
#pragma unroll
        for (int i = 0; i < 16; i += 2) {
          const int row = (i & 3) + 8 * (i >> 2);
          const u32 pk = cvtpk(c1[i], c1[i + 1]);
          const u32 px = pk ^ 0x80008000u;
          const u32 b0 = __builtin_bit_cast(u32, __builtin_fmaf(c0[i], NLOG2E, tiny));
          const u32 b1 = __builtin_bit_cast(u32, __builtin_fmaf(c0[i + 1], NLOG2E, tiny));
          u32 t0_, t1_;
          asm("v_sub_u32_sdwa %0, %1, sext(%2) dst_sel:DWORD dst_unused:UNUSED_PAD src0_sel:DWORD src1_sel:WORD_0" : "=v"(t0_) : "v"(b0), "v"(px));
          asm("v_sub_u32_sdwa %0, %1, sext(%2) dst_sel:DWORD dst_unused:UNUSED_PAD src0_sel:DWORD src1_sel:WORD_1" : "=v"(t1_) : "v"(b1), "v"(px));
          bst<STAUX>(tbr, T + row * 128, __builtin_amdgcn_perm(t0_, pk, 0x07060100u));
          bst<STAUX>(tbr, T + row * 128 + 128, __builtin_amdgcn_perm(t1_, pk, 0x07060302u));
        }
      } else {
        const u32 au = (u32)WS_AU + ((j * BPB + t) * 4 + g) * AU_UNIT;
        u32x4 qa, qu;
        u32x3 qb;
        u32x2 qv;
#pragma unroll
        for (int r2 = 0; r2 < 4; ++r2) {
          qa[r2] = cvtpk(c0[2 * r2] * NLOG2E, c0[2 * r2 + 1] * NLOG2E);
          qu[r2] = cvtpk(c1[2 * r2], c1[2 * r2 + 1]);
        }
        qb[0] = cvtpk(c0[8] * NLOG2E, c0[9] * NLOG2E);
        qb[1] = cvtpk(c0[10] * NLOG2E, c0[11] * NLOG2E);
        qb[2] = h ? 0u : cvtpk(c0[12] * NLOG2E, c1[12]);
        qv[0] = cvtpk(c1[8], c1[9]);
        qv[1] = cvtpk(c1[10], c1[11]);
        bst<STAUX>(wsr, au + lane * 16, qa);
        bst<STAUX>(wsr, au + 1024 + lane * 12, qb);
        bst<STAUX>(wsr, au + 1792 + lane * 16, qu);
        bst<STAUX>(wsr, au + 2816 + lane * 8, qv);
      }
    }
  }
}

constexpr int SLOT = 1536;
constexpr int EXN = WIN + 128;
constexpr int WAVE_LDS = 2 * SLOT + EXN * 4;
constexpr int L_LA = 4 * WAVE_LDS;
constexpr int L_LS = L_LA + 2048;
constexpr int L_TOTAL = L_LS + 28 * 256;
static_assert(L_TOTAL <= 40960, "LDS budget");
static_assert(WAVE_LDS >= NPB * 128, "epilogue tile");

#define K2_MAIN_ASM \
  "s_cmp_eq_u32 %[first], 0\n" \
  "s_cbranch_scc1 LZ_%=\n" \
  "v_mov_b32 v96, 0\n" \
  "v_mov_b32 v97, 0\n" \
  "v_mov_b32 v98, 0\n" \
  "v_mov_b32 v99, 0\n" \
  "s_nop 1\n" \
  "v_mfma_f32_32x32x16_bf16 v[0:15], v[96:99], v[96:99], 0\n" \
  "v_mfma_f32_32x32x16_bf16 v[16:31], v[96:99], v[96:99], 0\n" \
  "LZ_%=:\n" \
  "ds_read_b128 v[48:51], v113 offset:0\n" \
  "ds_read_b128 v[52:55], v113 offset:32\n" \
  "ds_read_b128 v[56:59], v113 offset:64\n" \
  "ds_read_b128 v[60:63], v113 offset:96\n" \
  "ds_read_b32 v94, v115 offset:0\n" \
  "ds_read_b32 v95, v115 offset:256\n" \
  "ds_read_b128 v[100:103], v114 offset:0\n" \
  "s_waitcnt lgkmcnt(6)\n" \
  "buffer_load_dword v48, v48, %[trs], 0 offen\n" \
  "buffer_load_dword v49, v49, %[trs], 0 offen\n" \
  "buffer_load_dword v50, v50, %[trs], 0 offen\n" \
  "buffer_load_dword v51, v51, %[trs], 0 offen\n" \
  "s_waitcnt lgkmcnt(5)\n" \
  "buffer_load_dword v52, v52, %[trs], 0 offen\n" \
  "buffer_load_dword v53, v53, %[trs], 0 offen\n" \
  "buffer_load_dword v54, v54, %[trs], 0 offen\n" \
  "buffer_load_dword v55, v55, %[trs], 0 offen\n" \
  "s_waitcnt lgkmcnt(4)\n" \
  "buffer_load_dword v56, v56, %[trs], 0 offen\n" \
  "buffer_load_dword v57, v57, %[trs], 0 offen\n" \
  "buffer_load_dword v58, v58, %[trs], 0 offen\n" \
  "buffer_load_dword v59, v59, %[trs], 0 offen\n" \
  "s_waitcnt lgkmcnt(3)\n" \
  "buffer_load_dword v60, v60, %[trs], 0 offen\n" \
  "buffer_load_dword v61, v61, %[trs], 0 offen\n" \
  "buffer_load_dword v62, v62, %[trs], 0 offen\n" \
  "buffer_load_dword v63, v63, %[trs], 0 offen\n" \
  "s_waitcnt lgkmcnt(1)\n" \
  "ds_read_b128 v[88:91], v94\n" \
  "ds_read_b96 v[92:94], v94 offset:512\n" \
  "s_cmp_eq_u32 %[rem], 1\n" \
  "s_cbranch_scc0 LE2_%=\n" \
  "v_mbcnt_lo_u32_b32 v118, -1, 0\n" \
  "v_mbcnt_hi_u32_b32 v118, -1, v118\n" \
  "v_lshlrev_b32 v119, 4, v118\n" \
  "global_load_dwordx4 v[72:75], v119, %[aub] offset:1792\n" \
  "v_lshlrev_b32 v120, 3, v118\n" \
  "global_load_dwordx2 v[76:77], v120, %[aub] offset:2816\n" \
  "v_mul_u32_u24 v121, 12, v118\n" \
  "global_load_dword v78, v121, %[aub] offset:1032\n" \
  "v_and_b32 v120, 3, v118\n" \
  "v_lshlrev_b32 v120, 4, v120\n" \
  "v_lshrrev_b32 v118, 2, v118\n" \
  "v_lshl_add_u32 v119, v118, 8, v120\n" \
  "global_load_dwordx4 v[64:67], v119, %[xb]\n" \
  "v_min_u32 v118, 8, v118\n" \
  "v_add_u32 v118, 16, v118\n" \
  "v_lshl_add_u32 v119, v118, 8, v120\n" \
  "global_load_dwordx4 v[68:71], v119, %[xb]\n" \
  "s_waitcnt lgkmcnt(0)\n" \
  "s_branch LL0_%=\n" \
  "LE2_%=:\n" \
  "ds_read_b128 v[64:67], v113 offset:128\n" \
  "ds_read_b128 v[68:71], v113 offset:160\n" \
  "ds_read_b128 v[72:75], v113 offset:192\n" \
  "ds_read_b128 v[76:79], v113 offset:224\n" \
  "s_waitcnt lgkmcnt(3)\n" \
  "buffer_load_dword v64, v64, %[trs], 0 offen\n" \
  "buffer_load_dword v65, v65, %[trs], 0 offen\n" \
  "buffer_load_dword v66, v66, %[trs], 0 offen\n" \
  "buffer_load_dword v67, v67, %[trs], 0 offen\n" \
  "s_waitcnt lgkmcnt(2)\n" \
  "buffer_load_dword v68, v68, %[trs], 0 offen\n" \
  "buffer_load_dword v69, v69, %[trs], 0 offen\n" \
  "buffer_load_dword v70, v70, %[trs], 0 offen\n" \
  "buffer_load_dword v71, v71, %[trs], 0 offen\n" \
  "s_waitcnt lgkmcnt(1)\n" \
  "buffer_load_dword v72, v72, %[trs], 0 offen\n" \
  "buffer_load_dword v73, v73, %[trs], 0 offen\n" \
  "buffer_load_dword v74, v74, %[trs], 0 offen\n" \
  "buffer_load_dword v75, v75, %[trs], 0 offen\n" \
  "s_waitcnt lgkmcnt(0)\n" \
  "buffer_load_dword v76, v76, %[trs], 0 offen\n" \
  "buffer_load_dword v77, v77, %[trs], 0 offen\n" \
  "buffer_load_dword v78, v78, %[trs], 0 offen\n" \
  "buffer_load_dword v79, v79, %[trs], 0 offen\n" \
  "LT0_%=:\n" \
  "s_setprio 0\n" \
  "s_waitcnt vmcnt(16)\n" \
  "v_mfma_f32_32x32x16_bf16 v[32:47], v[88:91], v[80:83], v[48:63]\n" \
  "v_mfma_f32_32x32x16_bf16 v[32:47], v[92:95], v[84:87], v[32:47]\n" \
  "ds_read_b32 v94, v115 offset:128\n" \
  "ds_read_b32 v95, v115 offset:384\n" \
  "ds_read_b32 v107, v108 offset:0\n" \
  "s_waitcnt lgkmcnt(3)\n" \
  "v_lshl_add_u64 v[100:101], v[100:101], 0, v[116:117]\n" \
  "v_lshl_add_u64 v[102:103], v[102:103], 0, v[116:117]\n" \
  "ds_read_b64_tr_b16 v[96:97], v100\n" \
  "ds_read_b64_tr_b16 v[98:99], v101\n" \
  "ds_read_b64_tr_b16 v[100:101], v102\n" \
  "ds_read_b64_tr_b16 v[102:103], v103\n" \
  "s_waitcnt lgkmcnt(4)\n" \
  "ds_read_b128 v[88:91], v94\n" \
  "ds_read_b96 v[92:94], v94 offset:512\n" \
  "s_cmp_gt_u32 %[rem], 2\n" \
  "s_cbranch_scc0 LS0_%=\n" \
  "v_and_b32 v118, 31, v104\n" \
  "v_and_b32 v119, 0xffffff80, v104\n" \
  "v_add_u32 v119, v119, v111\n" \
  "v_lshl_add_u32 v120, v118, 4, %[lab]\n" \
  "v_lshl_add_u32 v121, v118, 4, %[lab1]\n" \
  "v_lshl_add_u32 v118, v118, 8, v112\n" \
  "ds_write2st64_b32 v109, v119, v120 offset0:6 offset1:8\n" \
  "ds_write2st64_b32 v109, v106, v121 offset0:9 offset1:10\n" \
  "ds_write_b32 v109, v105 offset:2816\n" \
  "ds_write_b32 v110, v118 offset:1536\n" \
  "buffer_load_dwordx3 v[104:106], v107, %[rrs], 0 offen\n" \
  "LS0_%=:\n" \
  "s_setprio 1\n" \
  "v_exp_f32 v32, v32\n" \
  "v_exp_f32 v33, v33\n" \
  "v_exp_f32 v34, v34\n" \
  "v_exp_f32 v35, v35\n" \
  "v_exp_f32 v36, v36\n" \
  "v_exp_f32 v37, v37\n" \
  "v_exp_f32 v38, v38\n" \
  "v_exp_f32 v39, v39\n" \
  "v_exp_f32 v40, v40\n" \
  "v_exp_f32 v41, v41\n" \
  "v_exp_f32 v42, v42\n" \
  "v_exp_f32 v43, v43\n" \
  "v_exp_f32 v44, v44\n" \
  "v_exp_f32 v45, v45\n" \
  "v_exp_f32 v46, v46\n" \
  "v_exp_f32 v47, v47\n" \
  "s_nop 1\n" \
  "v_mfma_f32_32x32x16_bf16 v[32:47], v[122:125], v[122:125], v[32:47]\n" \
  "s_setprio 2\n" \
  "v_lshlrev_b32 v48, 16, v48\n" \
  "v_lshlrev_b32 v49, 16, v49\n" \
  "v_lshlrev_b32 v50, 16, v50\n" \
  "v_lshlrev_b32 v51, 16, v51\n" \
  "v_lshlrev_b32 v52, 16, v52\n" \
  "v_lshlrev_b32 v53, 16, v53\n" \
  "v_lshlrev_b32 v54, 16, v54\n" \
  "v_lshlrev_b32 v55, 16, v55\n" \
  "v_lshlrev_b32 v56, 16, v56\n" \
  "v_lshlrev_b32 v57, 16, v57\n" \
  "v_lshlrev_b32 v58, 16, v58\n" \
  "v_lshlrev_b32 v59, 16, v59\n" \
  "v_lshlrev_b32 v60, 16, v60\n" \
  "v_lshlrev_b32 v61, 16, v61\n" \
  "v_lshlrev_b32 v62, 16, v62\n" \
  "v_lshlrev_b32 v63, 16, v63\n" \
  "v_rcp_f32 v32, v32\n" \
  "v_rcp_f32 v33, v33\n" \
  "v_rcp_f32 v34, v34\n" \
  "v_rcp_f32 v35, v35\n" \
  "v_rcp_f32 v36, v36\n" \
  "v_rcp_f32 v37, v37\n" \
  "v_rcp_f32 v38, v38\n" \
  "v_rcp_f32 v39, v39\n" \
  "v_rcp_f32 v40, v40\n" \
  "v_rcp_f32 v41, v41\n" \
  "v_rcp_f32 v42, v42\n" \
  "v_rcp_f32 v43, v43\n" \
  "v_rcp_f32 v44, v44\n" \
  "v_rcp_f32 v45, v45\n" \
  "v_rcp_f32 v46, v46\n" \
  "v_rcp_f32 v47, v47\n" \
  "s_setprio 3\n" \
  "v_pk_mul_f32 v[48:49], v[32:33], v[48:49]\n" \
  "v_pk_mul_f32 v[50:51], v[34:35], v[50:51]\n" \
  "v_pk_mul_f32 v[52:53], v[36:37], v[52:53]\n" \
  "v_pk_mul_f32 v[54:55], v[38:39], v[54:55]\n" \
  "v_pk_mul_f32 v[56:57], v[40:41], v[56:57]\n" \
  "v_pk_mul_f32 v[58:59], v[42:43], v[58:59]\n" \
  "v_pk_mul_f32 v[60:61], v[44:45], v[60:61]\n" \
  "v_pk_mul_f32 v[62:63], v[46:47], v[62:63]\n" \
  "v_cvt_pk_bf16_f32 v32, v32, v33\n" \
  "v_cvt_pk_bf16_f32 v33, v34, v35\n" \
  "v_cvt_pk_bf16_f32 v34, v36, v37\n" \
  "v_cvt_pk_bf16_f32 v35, v38, v39\n" \
  "v_cvt_pk_bf16_f32 v36, v48, v49\n" \
  "v_cvt_pk_bf16_f32 v37, v50, v51\n" \
  "v_cvt_pk_bf16_f32 v38, v52, v53\n" \
  "v_cvt_pk_bf16_f32 v39, v54, v55\n" \
  "v_cvt_pk_bf16_f32 v40, v40, v41\n" \
  "v_cvt_pk_bf16_f32 v41, v42, v43\n" \
  "v_cvt_pk_bf16_f32 v42, v44, v45\n" \
  "v_cvt_pk_bf16_f32 v43, v46, v47\n" \
  "v_cvt_pk_bf16_f32 v44, v56, v57\n" \
  "v_cvt_pk_bf16_f32 v45, v58, v59\n" \
  "v_cvt_pk_bf16_f32 v46, v60, v61\n" \
  "v_cvt_pk_bf16_f32 v47, v62, v63\n" \
  "s_cmp_eq_u32 %[rem], 2\n" \
  "s_cbranch_scc1 LP0_%=\n" \
  "ds_read_b128 v[48:51], v113 offset:1536\n" \
  "ds_read_b128 v[52:55], v113 offset:1568\n" \
  "ds_read_b128 v[56:59], v113 offset:1600\n" \
  "ds_read_b128 v[60:63], v113 offset:1632\n" \
  "s_waitcnt lgkmcnt(4)\n" \
  "v_mfma_f32_32x32x16_bf16 v[16:31], v[96:99], v[32:35], v[16:31]\n" \
  "v_mfma_f32_32x32x16_bf16 v[0:15], v[96:99], v[36:39], v[0:15]\n" \
  "v_mfma_f32_32x32x16_bf16 v[16:31], v[100:103], v[40:43], v[16:31]\n" \
  "v_mfma_f32_32x32x16_bf16 v[0:15], v[100:103], v[44:47], v[0:15]\n" \
  "ds_read_b128 v[100:103], v114 offset:128\n" \
  "s_waitcnt lgkmcnt(4)\n" \
  "buffer_load_dword v48, v48, %[trs], 0 offen\n" \
  "buffer_load_dword v49, v49, %[trs], 0 offen\n" \
  "buffer_load_dword v50, v50, %[trs], 0 offen\n" \
  "buffer_load_dword v51, v51, %[trs], 0 offen\n" \
  "s_waitcnt lgkmcnt(3)\n" \
  "buffer_load_dword v52, v52, %[trs], 0 offen\n" \
  "buffer_load_dword v53, v53, %[trs], 0 offen\n" \
  "buffer_load_dword v54, v54, %[trs], 0 offen\n" \
  "buffer_load_dword v55, v55, %[trs], 0 offen\n" \
  "s_waitcnt lgkmcnt(2)\n" \
  "buffer_load_dword v56, v56, %[trs], 0 offen\n" \
  "buffer_load_dword v57, v57, %[trs], 0 offen\n" \
  "buffer_load_dword v58, v58, %[trs], 0 offen\n" \
  "buffer_load_dword v59, v59, %[trs], 0 offen\n" \
  "s_waitcnt lgkmcnt(1)\n" \
  "buffer_load_dword v60, v60, %[trs], 0 offen\n" \
  "buffer_load_dword v61, v61, %[trs], 0 offen\n" \
  "buffer_load_dword v62, v62, %[trs], 0 offen\n" \
  "buffer_load_dword v63, v63, %[trs], 0 offen\n" \
  "s_sub_u32 %[rem], %[rem], 1\n" \
  "LT1_%=:\n" \
  "s_setprio 0\n" \
  "s_waitcnt vmcnt(17)\n" \
  "v_mfma_f32_32x32x16_bf16 v[32:47], v[88:91], v[80:83], v[64:79]\n" \
  "v_mfma_f32_32x32x16_bf16 v[32:47], v[92:95], v[84:87], v[32:47]\n" \
  "ds_read_b32 v94, v115 offset:1536\n" \
  "ds_read_b32 v95, v115 offset:1792\n" \
  "s_waitcnt lgkmcnt(2)\n" \
  "v_lshl_add_u64 v[100:101], v[100:101], 0, v[116:117]\n" \
  "v_lshl_add_u64 v[102:103], v[102:103], 0, v[116:117]\n" \
  "ds_read_b64_tr_b16 v[96:97], v100\n" \
  "ds_read_b64_tr_b16 v[98:99], v101\n" \
  "ds_read_b64_tr_b16 v[100:101], v102\n" \
  "ds_read_b64_tr_b16 v[102:103], v103\n" \
  "s_waitcnt lgkmcnt(4)\n" \
  "ds_read_b128 v[88:91], v94\n" \
  "ds_read_b96 v[92:94], v94 offset:512\n" \
  "s_setprio 1\n" \
  "v_exp_f32 v32, v32\n" \
  "v_exp_f32 v33, v33\n" \
  "v_exp_f32 v34, v34\n" \
  "v_exp_f32 v35, v35\n" \
  "v_exp_f32 v36, v36\n" \
  "v_exp_f32 v37, v37\n" \
  "v_exp_f32 v38, v38\n" \
  "v_exp_f32 v39, v39\n" \
  "v_exp_f32 v40, v40\n" \
  "v_exp_f32 v41, v41\n" \
  "v_exp_f32 v42, v42\n" \
  "v_exp_f32 v43, v43\n" \
  "v_exp_f32 v44, v44\n" \
  "v_exp_f32 v45, v45\n" \
  "v_exp_f32 v46, v46\n" \
  "v_exp_f32 v47, v47\n" \
  "s_nop 1\n" \
  "v_mfma_f32_32x32x16_bf16 v[32:47], v[122:125], v[122:125], v[32:47]\n" \
  "s_setprio 2\n" \
  "v_lshlrev_b32 v64, 16, v64\n" \
  "v_lshlrev_b32 v65, 16, v65\n" \
  "v_lshlrev_b32 v66, 16, v66\n" \
  "v_lshlrev_b32 v67, 16, v67\n" \
  "v_lshlrev_b32 v68, 16, v68\n" \
  "v_lshlrev_b32 v69, 16, v69\n" \
  "v_lshlrev_b32 v70, 16, v70\n" \
  "v_lshlrev_b32 v71, 16, v71\n" \
  "v_lshlrev_b32 v72, 16, v72\n" \
  "v_lshlrev_b32 v73, 16, v73\n" \
  "v_lshlrev_b32 v74, 16, v74\n" \
  "v_lshlrev_b32 v75, 16, v75\n" \
  "v_lshlrev_b32 v76, 16, v76\n" \
  "v_lshlrev_b32 v77, 16, v77\n" \
  "v_lshlrev_b32 v78, 16, v78\n" \
  "v_lshlrev_b32 v79, 16, v79\n" \
  "v_rcp_f32 v32, v32\n" \
  "v_rcp_f32 v33, v33\n" \
  "v_rcp_f32 v34, v34\n" \
  "v_rcp_f32 v35, v35\n" \
  "v_rcp_f32 v36, v36\n" \
  "v_rcp_f32 v37, v37\n" \
  "v_rcp_f32 v38, v38\n" \
  "v_rcp_f32 v39, v39\n" \
  "v_rcp_f32 v40, v40\n" \
  "v_rcp_f32 v41, v41\n" \
  "v_rcp_f32 v42, v42\n" \
  "v_rcp_f32 v43, v43\n" \
  "v_rcp_f32 v44, v44\n" \
  "v_rcp_f32 v45, v45\n" \
  "v_rcp_f32 v46, v46\n" \
  "v_rcp_f32 v47, v47\n" \
  "s_setprio 3\n" \
  "v_pk_mul_f32 v[64:65], v[32:33], v[64:65]\n" \
  "v_pk_mul_f32 v[66:67], v[34:35], v[66:67]\n" \
  "v_pk_mul_f32 v[68:69], v[36:37], v[68:69]\n" \
  "v_pk_mul_f32 v[70:71], v[38:39], v[70:71]\n" \
  "v_pk_mul_f32 v[72:73], v[40:41], v[72:73]\n" \
  "v_pk_mul_f32 v[74:75], v[42:43], v[74:75]\n" \
  "v_pk_mul_f32 v[76:77], v[44:45], v[76:77]\n" \
  "v_pk_mul_f32 v[78:79], v[46:47], v[78:79]\n" \
  "v_cvt_pk_bf16_f32 v32, v32, v33\n" \
  "v_cvt_pk_bf16_f32 v33, v34, v35\n" \
  "v_cvt_pk_bf16_f32 v34, v36, v37\n" \
  "v_cvt_pk_bf16_f32 v35, v38, v39\n" \
  "v_cvt_pk_bf16_f32 v36, v64, v65\n" \
  "v_cvt_pk_bf16_f32 v37, v66, v67\n" \
  "v_cvt_pk_bf16_f32 v38, v68, v69\n" \
  "v_cvt_pk_bf16_f32 v39, v70, v71\n" \
  "v_cvt_pk_bf16_f32 v40, v40, v41\n" \
  "v_cvt_pk_bf16_f32 v41, v42, v43\n" \
  "v_cvt_pk_bf16_f32 v42, v44, v45\n" \
  "v_cvt_pk_bf16_f32 v43, v46, v47\n" \
  "v_cvt_pk_bf16_f32 v44, v72, v73\n" \
  "v_cvt_pk_bf16_f32 v45, v74, v75\n" \
  "v_cvt_pk_bf16_f32 v46, v76, v77\n" \
  "v_cvt_pk_bf16_f32 v47, v78, v79\n" \
  "s_cmp_eq_u32 %[rem], 2\n" \
  "s_cbranch_scc1 LP1_%=\n" \
  "ds_read_b128 v[64:67], v113 offset:1664\n" \
  "ds_read_b128 v[68:71], v113 offset:1696\n" \
  "ds_read_b128 v[72:75], v113 offset:1728\n" \
  "ds_read_b128 v[76:79], v113 offset:1760\n" \
  "s_waitcnt lgkmcnt(4)\n" \
  "v_mfma_f32_32x32x16_bf16 v[16:31], v[96:99], v[32:35], v[16:31]\n" \
  "v_mfma_f32_32x32x16_bf16 v[0:15], v[96:99], v[36:39], v[0:15]\n" \
  "v_mfma_f32_32x32x16_bf16 v[16:31], v[100:103], v[40:43], v[16:31]\n" \
  "v_mfma_f32_32x32x16_bf16 v[0:15], v[100:103], v[44:47], v[0:15]\n" \
  "ds_read_b128 v[100:103], v114 offset:1536\n" \
  "s_waitcnt lgkmcnt(4)\n" \
  "buffer_load_dword v64, v64, %[trs], 0 offen\n" \
  "buffer_load_dword v65, v65, %[trs], 0 offen\n" \
  "buffer_load_dword v66, v66, %[trs], 0 offen\n" \
  "buffer_load_dword v67, v67, %[trs], 0 offen\n" \
  "s_waitcnt lgkmcnt(3)\n" \
  "buffer_load_dword v68, v68, %[trs], 0 offen\n" \
  "buffer_load_dword v69, v69, %[trs], 0 offen\n" \
  "buffer_load_dword v70, v70, %[trs], 0 offen\n" \
  "buffer_load_dword v71, v71, %[trs], 0 offen\n" \
  "s_waitcnt lgkmcnt(2)\n" \
  "buffer_load_dword v72, v72, %[trs], 0 offen\n" \
  "buffer_load_dword v73, v73, %[trs], 0 offen\n" \
  "buffer_load_dword v74, v74, %[trs], 0 offen\n" \
  "buffer_load_dword v75, v75, %[trs], 0 offen\n" \
  "s_waitcnt lgkmcnt(1)\n" \
  "buffer_load_dword v76, v76, %[trs], 0 offen\n" \
  "buffer_load_dword v77, v77, %[trs], 0 offen\n" \
  "buffer_load_dword v78, v78, %[trs], 0 offen\n" \
  "buffer_load_dword v79, v79, %[trs], 0 offen\n" \
  "s_sub_u32 %[rem], %[rem], 1\n" \
  "LT2_%=:\n" \
  "s_setprio 0\n" \
  "s_waitcnt vmcnt(16)\n" \
  "v_mfma_f32_32x32x16_bf16 v[32:47], v[88:91], v[80:83], v[48:63]\n" \
  "v_mfma_f32_32x32x16_bf16 v[32:47], v[92:95], v[84:87], v[32:47]\n" \
  "ds_read_b32 v94, v115 offset:1664\n" \
  "ds_read_b32 v95, v115 offset:1920\n" \
  "ds_read_b32 v107, v108 offset:256\n" \
  "s_waitcnt lgkmcnt(3)\n" \
  "v_lshl_add_u64 v[100:101], v[100:101], 0, v[116:117]\n" \
  "v_lshl_add_u64 v[102:103], v[102:103], 0, v[116:117]\n" \
  "ds_read_b64_tr_b16 v[96:97], v100\n" \
  "ds_read_b64_tr_b16 v[98:99], v101\n" \
  "ds_read_b64_tr_b16 v[100:101], v102\n" \
  "ds_read_b64_tr_b16 v[102:103], v103\n" \
  "s_waitcnt lgkmcnt(4)\n" \
  "ds_read_b128 v[88:91], v94\n" \
  "ds_read_b96 v[92:94], v94 offset:512\n" \
  "s_cmp_gt_u32 %[rem], 2\n" \
  "s_cbranch_scc0 LS2_%=\n" \
  "v_and_b32 v118, 31, v104\n" \
  "v_and_b32 v119, 0xffffff80, v104\n" \
  "v_add_u32 v119, v119, v111\n" \
  "v_lshl_add_u32 v120, v118, 4, %[lab]\n" \
  "v_lshl_add_u32 v121, v118, 4, %[lab1]\n" \
  "v_lshl_add_u32 v118, v118, 8, v112\n" \
  "ds_write2st64_b32 v109, v119, v120 offset0:0 offset1:2\n" \
  "ds_write2st64_b32 v109, v106, v121 offset0:3 offset1:4\n" \
  "ds_write_b32 v109, v105 offset:1280\n" \
  "ds_write_b32 v110, v118 offset:0\n" \
  "buffer_load_dwordx3 v[104:106], v107, %[rrs], 0 offen\n" \
  "v_add_u32 v108, 0x200, v108\n" \
  "LS2_%=:\n" \
  "s_setprio 1\n" \
  "v_exp_f32 v32, v32\n" \
  "v_exp_f32 v33, v33\n" \
  "v_exp_f32 v34, v34\n" \
  "v_exp_f32 v35, v35\n" \
  "v_exp_f32 v36, v36\n" \
  "v_exp_f32 v37, v37\n" \
  "v_exp_f32 v38, v38\n" \
  "v_exp_f32 v39, v39\n" \
  "v_exp_f32 v40, v40\n" \
  "v_exp_f32 v41, v41\n" \
  "v_exp_f32 v42, v42\n" \
  "v_exp_f32 v43, v43\n" \
  "v_exp_f32 v44, v44\n" \
  "v_exp_f32 v45, v45\n" \
  "v_exp_f32 v46, v46\n" \
  "v_exp_f32 v47, v47\n" \
  "s_nop 1\n" \
  "v_mfma_f32_32x32x16_bf16 v[32:47], v[122:125], v[122:125], v[32:47]\n" \
  "s_setprio 2\n" \
  "v_lshlrev_b32 v48, 16, v48\n" \
  "v_lshlrev_b32 v49, 16, v49\n" \
  "v_lshlrev_b32 v50, 16, v50\n" \
  "v_lshlrev_b32 v51, 16, v51\n" \
  "v_lshlrev_b32 v52, 16, v52\n" \
  "v_lshlrev_b32 v53, 16, v53\n" \
  "v_lshlrev_b32 v54, 16, v54\n" \
  "v_lshlrev_b32 v55, 16, v55\n" \
  "v_lshlrev_b32 v56, 16, v56\n" \
  "v_lshlrev_b32 v57, 16, v57\n" \
  "v_lshlrev_b32 v58, 16, v58\n" \
  "v_lshlrev_b32 v59, 16, v59\n" \
  "v_lshlrev_b32 v60, 16, v60\n" \
  "v_lshlrev_b32 v61, 16, v61\n" \
  "v_lshlrev_b32 v62, 16, v62\n" \
  "v_lshlrev_b32 v63, 16, v63\n" \
  "v_rcp_f32 v32, v32\n" \
  "v_rcp_f32 v33, v33\n" \
  "v_rcp_f32 v34, v34\n" \
  "v_rcp_f32 v35, v35\n" \
  "v_rcp_f32 v36, v36\n" \
  "v_rcp_f32 v37, v37\n" \
  "v_rcp_f32 v38, v38\n" \
  "v_rcp_f32 v39, v39\n" \
  "v_rcp_f32 v40, v40\n" \
  "v_rcp_f32 v41, v41\n" \
  "v_rcp_f32 v42, v42\n" \
  "v_rcp_f32 v43, v43\n" \
  "v_rcp_f32 v44, v44\n" \
  "v_rcp_f32 v45, v45\n" \
  "v_rcp_f32 v46, v46\n" \
  "v_rcp_f32 v47, v47\n" \
  "s_setprio 3\n" \
  "v_pk_mul_f32 v[48:49], v[32:33], v[48:49]\n" \
  "v_pk_mul_f32 v[50:51], v[34:35], v[50:51]\n" \
  "v_pk_mul_f32 v[52:53], v[36:37], v[52:53]\n" \
  "v_pk_mul_f32 v[54:55], v[38:39], v[54:55]\n" \
  "v_pk_mul_f32 v[56:57], v[40:41], v[56:57]\n" \
  "v_pk_mul_f32 v[58:59], v[42:43], v[58:59]\n" \
  "v_pk_mul_f32 v[60:61], v[44:45], v[60:61]\n" \
  "v_pk_mul_f32 v[62:63], v[46:47], v[62:63]\n" \
  "v_cvt_pk_bf16_f32 v32, v32, v33\n" \
  "v_cvt_pk_bf16_f32 v33, v34, v35\n" \
  "v_cvt_pk_bf16_f32 v34, v36, v37\n" \
  "v_cvt_pk_bf16_f32 v35, v38, v39\n" \
  "v_cvt_pk_bf16_f32 v36, v48, v49\n" \
  "v_cvt_pk_bf16_f32 v37, v50, v51\n" \
  "v_cvt_pk_bf16_f32 v38, v52, v53\n" \
  "v_cvt_pk_bf16_f32 v39, v54, v55\n" \
  "v_cvt_pk_bf16_f32 v40, v40, v41\n" \
  "v_cvt_pk_bf16_f32 v41, v42, v43\n" \
  "v_cvt_pk_bf16_f32 v42, v44, v45\n" \
  "v_cvt_pk_bf16_f32 v43, v46, v47\n" \
  "v_cvt_pk_bf16_f32 v44, v56, v57\n" \
  "v_cvt_pk_bf16_f32 v45, v58, v59\n" \
  "v_cvt_pk_bf16_f32 v46, v60, v61\n" \
  "v_cvt_pk_bf16_f32 v47, v62, v63\n" \
  "s_cmp_eq_u32 %[rem], 2\n" \
  "s_cbranch_scc1 LP2_%=\n" \
  "ds_read_b128 v[48:51], v113 offset:0\n" \
  "ds_read_b128 v[52:55], v113 offset:32\n" \
  "ds_read_b128 v[56:59], v113 offset:64\n" \
  "ds_read_b128 v[60:63], v113 offset:96\n" \
  "s_waitcnt lgkmcnt(4)\n" \
  "v_mfma_f32_32x32x16_bf16 v[16:31], v[96:99], v[32:35], v[16:31]\n" \
  "v_mfma_f32_32x32x16_bf16 v[0:15], v[96:99], v[36:39], v[0:15]\n" \
  "v_mfma_f32_32x32x16_bf16 v[16:31], v[100:103], v[40:43], v[16:31]\n" \
  "v_mfma_f32_32x32x16_bf16 v[0:15], v[100:103], v[44:47], v[0:15]\n" \
  "ds_read_b128 v[100:103], v114 offset:1664\n" \
  "s_waitcnt lgkmcnt(4)\n" \
  "buffer_load_dword v48, v48, %[trs], 0 offen\n" \
  "buffer_load_dword v49, v49, %[trs], 0 offen\n" \
  "buffer_load_dword v50, v50, %[trs], 0 offen\n" \
  "buffer_load_dword v51, v51, %[trs], 0 offen\n" \
  "s_waitcnt lgkmcnt(3)\n" \
  "buffer_load_dword v52, v52, %[trs], 0 offen\n" \
  "buffer_load_dword v53, v53, %[trs], 0 offen\n" \
  "buffer_load_dword v54, v54, %[trs], 0 offen\n" \
  "buffer_load_dword v55, v55, %[trs], 0 offen\n" \
  "s_waitcnt lgkmcnt(2)\n" \
  "buffer_load_dword v56, v56, %[trs], 0 offen\n" \
  "buffer_load_dword v57, v57, %[trs], 0 offen\n" \
  "buffer_load_dword v58, v58, %[trs], 0 offen\n" \
  "buffer_load_dword v59, v59, %[trs], 0 offen\n" \
  "s_waitcnt lgkmcnt(1)\n" \
  "buffer_load_dword v60, v60, %[trs], 0 offen\n" \
  "buffer_load_dword v61, v61, %[trs], 0 offen\n" \
  "buffer_load_dword v62, v62, %[trs], 0 offen\n" \
  "buffer_load_dword v63, v63, %[trs], 0 offen\n" \
  "s_sub_u32 %[rem], %[rem], 1\n" \
  "LT3_%=:\n" \
  "s_setprio 0\n" \
  "s_waitcnt vmcnt(17)\n" \
  "v_mfma_f32_32x32x16_bf16 v[32:47], v[88:91], v[80:83], v[64:79]\n" \
  "v_mfma_f32_32x32x16_bf16 v[32:47], v[92:95], v[84:87], v[32:47]\n" \
  "ds_read_b32 v94, v115 offset:0\n" \
  "ds_read_b32 v95, v115 offset:256\n" \
  "s_waitcnt lgkmcnt(2)\n" \
  "v_lshl_add_u64 v[100:101], v[100:101], 0, v[116:117]\n" \
  "v_lshl_add_u64 v[102:103], v[102:103], 0, v[116:117]\n" \
  "ds_read_b64_tr_b16 v[96:97], v100\n" \
  "ds_read_b64_tr_b16 v[98:99], v101\n" \
  "ds_read_b64_tr_b16 v[100:101], v102\n" \
  "ds_read_b64_tr_b16 v[102:103], v103\n" \
  "s_waitcnt lgkmcnt(4)\n" \
  "ds_read_b128 v[88:91], v94\n" \
  "ds_read_b96 v[92:94], v94 offset:512\n" \
  "s_setprio 1\n" \
  "v_exp_f32 v32, v32\n" \
  "v_exp_f32 v33, v33\n" \
  "v_exp_f32 v34, v34\n" \
  "v_exp_f32 v35, v35\n" \
  "v_exp_f32 v36, v36\n" \
  "v_exp_f32 v37, v37\n" \
  "v_exp_f32 v38, v38\n" \
  "v_exp_f32 v39, v39\n" \
  "v_exp_f32 v40, v40\n" \
  "v_exp_f32 v41, v41\n" \
  "v_exp_f32 v42, v42\n" \
  "v_exp_f32 v43, v43\n" \
  "v_exp_f32 v44, v44\n" \
  "v_exp_f32 v45, v45\n" \
  "v_exp_f32 v46, v46\n" \
  "v_exp_f32 v47, v47\n" \
  "s_nop 1\n" \
  "v_mfma_f32_32x32x16_bf16 v[32:47], v[122:125], v[122:125], v[32:47]\n" \
  "s_setprio 2\n" \
  "v_lshlrev_b32 v64, 16, v64\n" \
  "v_lshlrev_b32 v65, 16, v65\n" \
  "v_lshlrev_b32 v66, 16, v66\n" \
  "v_lshlrev_b32 v67, 16, v67\n" \
  "v_lshlrev_b32 v68, 16, v68\n" \
  "v_lshlrev_b32 v69, 16, v69\n" \
  "v_lshlrev_b32 v70, 16, v70\n" \
  "v_lshlrev_b32 v71, 16, v71\n" \
  "v_lshlrev_b32 v72, 16, v72\n" \
  "v_lshlrev_b32 v73, 16, v73\n" \
  "v_lshlrev_b32 v74, 16, v74\n" \
  "v_lshlrev_b32 v75, 16, v75\n" \
  "v_lshlrev_b32 v76, 16, v76\n" \
  "v_lshlrev_b32 v77, 16, v77\n" \
  "v_lshlrev_b32 v78, 16, v78\n" \
  "v_lshlrev_b32 v79, 16, v79\n" \
  "v_rcp_f32 v32, v32\n" \
  "v_rcp_f32 v33, v33\n" \
  "v_rcp_f32 v34, v34\n" \
  "v_rcp_f32 v35, v35\n" \
  "v_rcp_f32 v36, v36\n" \
  "v_rcp_f32 v37, v37\n" \
  "v_rcp_f32 v38, v38\n" \
  "v_rcp_f32 v39, v39\n" \
  "v_rcp_f32 v40, v40\n" \
  "v_rcp_f32 v41, v41\n" \
  "v_rcp_f32 v42, v42\n" \
  "v_rcp_f32 v43, v43\n" \
  "v_rcp_f32 v44, v44\n" \
  "v_rcp_f32 v45, v45\n" \
  "v_rcp_f32 v46, v46\n" \
  "v_rcp_f32 v47, v47\n" \
  "s_setprio 3\n" \
  "v_pk_mul_f32 v[64:65], v[32:33], v[64:65]\n" \
  "v_pk_mul_f32 v[66:67], v[34:35], v[66:67]\n" \
  "v_pk_mul_f32 v[68:69], v[36:37], v[68:69]\n" \
  "v_pk_mul_f32 v[70:71], v[38:39], v[70:71]\n" \
  "v_pk_mul_f32 v[72:73], v[40:41], v[72:73]\n" \
  "v_pk_mul_f32 v[74:75], v[42:43], v[74:75]\n" \
  "v_pk_mul_f32 v[76:77], v[44:45], v[76:77]\n" \
  "v_pk_mul_f32 v[78:79], v[46:47], v[78:79]\n" \
  "v_cvt_pk_bf16_f32 v32, v32, v33\n" \
  "v_cvt_pk_bf16_f32 v33, v34, v35\n" \
  "v_cvt_pk_bf16_f32 v34, v36, v37\n" \
  "v_cvt_pk_bf16_f32 v35, v38, v39\n" \
  "v_cvt_pk_bf16_f32 v36, v64, v65\n" \
  "v_cvt_pk_bf16_f32 v37, v66, v67\n" \
  "v_cvt_pk_bf16_f32 v38, v68, v69\n" \
  "v_cvt_pk_bf16_f32 v39, v70, v71\n" \
  "v_cvt_pk_bf16_f32 v40, v40, v41\n" \
  "v_cvt_pk_bf16_f32 v41, v42, v43\n" \
  "v_cvt_pk_bf16_f32 v42, v44, v45\n" \
  "v_cvt_pk_bf16_f32 v43, v46, v47\n" \
  "v_cvt_pk_bf16_f32 v44, v72, v73\n" \
  "v_cvt_pk_bf16_f32 v45, v74, v75\n" \
  "v_cvt_pk_bf16_f32 v46, v76, v77\n" \
  "v_cvt_pk_bf16_f32 v47, v78, v79\n" \
  "s_cmp_eq_u32 %[rem], 2\n" \
  "s_cbranch_scc1 LP3_%=\n" \
  "ds_read_b128 v[64:67], v113 offset:128\n" \
  "ds_read_b128 v[68:71], v113 offset:160\n" \
  "ds_read_b128 v[72:75], v113 offset:192\n" \
  "ds_read_b128 v[76:79], v113 offset:224\n" \
  "s_waitcnt lgkmcnt(4)\n" \
  "v_mfma_f32_32x32x16_bf16 v[16:31], v[96:99], v[32:35], v[16:31]\n" \
  "v_mfma_f32_32x32x16_bf16 v[0:15], v[96:99], v[36:39], v[0:15]\n" \
  "v_mfma_f32_32x32x16_bf16 v[16:31], v[100:103], v[40:43], v[16:31]\n" \
  "v_mfma_f32_32x32x16_bf16 v[0:15], v[100:103], v[44:47], v[0:15]\n" \
  "ds_read_b128 v[100:103], v114 offset:0\n" \
  "s_waitcnt lgkmcnt(4)\n" \
  "buffer_load_dword v64, v64, %[trs], 0 offen\n" \
  "buffer_load_dword v65, v65, %[trs], 0 offen\n" \
  "buffer_load_dword v66, v66, %[trs], 0 offen\n" \
  "buffer_load_dword v67, v67, %[trs], 0 offen\n" \
  "s_waitcnt lgkmcnt(3)\n" \
  "buffer_load_dword v68, v68, %[trs], 0 offen\n" \
  "buffer_load_dword v69, v69, %[trs], 0 offen\n" \
  "buffer_load_dword v70, v70, %[trs], 0 offen\n" \
  "buffer_load_dword v71, v71, %[trs], 0 offen\n" \
  "s_waitcnt lgkmcnt(2)\n" \
  "buffer_load_dword v72, v72, %[trs], 0 offen\n" \
  "buffer_load_dword v73, v73, %[trs], 0 offen\n" \
  "buffer_load_dword v74, v74, %[trs], 0 offen\n" \
  "buffer_load_dword v75, v75, %[trs], 0 offen\n" \
  "s_waitcnt lgkmcnt(1)\n" \
  "buffer_load_dword v76, v76, %[trs], 0 offen\n" \
  "buffer_load_dword v77, v77, %[trs], 0 offen\n" \
  "buffer_load_dword v78, v78, %[trs], 0 offen\n" \
  "buffer_load_dword v79, v79, %[trs], 0 offen\n" \
  "s_sub_u32 %[rem], %[rem], 1\n" \
  "s_branch LT0_%=\n" \
  "LP0_%=:\n" \
  "s_waitcnt lgkmcnt(0)\n" \
  "v_mfma_f32_32x32x16_bf16 v[16:31], v[96:99], v[32:35], v[16:31]\n" \
  "v_mfma_f32_32x32x16_bf16 v[0:15], v[96:99], v[36:39], v[0:15]\n" \
  "v_mfma_f32_32x32x16_bf16 v[16:31], v[100:103], v[40:43], v[16:31]\n" \
  "v_mfma_f32_32x32x16_bf16 v[0:15], v[100:103], v[44:47], v[0:15]\n" \
  "ds_read_b128 v[100:103], v114 offset:128\n" \
  "v_mbcnt_lo_u32_b32 v118, -1, 0\n" \
  "v_mbcnt_hi_u32_b32 v118, -1, v118\n" \
  "v_lshlrev_b32 v119, 4, v118\n" \
  "global_load_dwordx4 v[56:59], v119, %[aub] offset:1792\n" \
  "v_lshlrev_b32 v120, 3, v118\n" \
  "global_load_dwordx2 v[60:61], v120, %[aub] offset:2816\n" \
  "v_mul_u32_u24 v121, 12, v118\n" \
  "global_load_dword v62, v121, %[aub] offset:1032\n" \
  "v_and_b32 v120, 3, v118\n" \
  "v_lshlrev_b32 v120, 4, v120\n" \
  "v_lshrrev_b32 v118, 2, v118\n" \
  "v_lshl_add_u32 v119, v118, 8, v120\n" \
  "global_load_dwordx4 v[48:51], v119, %[xb]\n" \
  "v_min_u32 v118, 8, v118\n" \
  "v_add_u32 v118, 16, v118\n" \
  "v_lshl_add_u32 v119, v118, 8, v120\n" \
  "global_load_dwordx4 v[52:55], v119, %[xb]\n" \
  "s_branch LL1_%=\n" \
  "LP1_%=:\n" \
  "s_waitcnt lgkmcnt(0)\n" \
  "v_mfma_f32_32x32x16_bf16 v[16:31], v[96:99], v[32:35], v[16:31]\n" \
  "v_mfma_f32_32x32x16_bf16 v[0:15], v[96:99], v[36:39], v[0:15]\n" \
  "v_mfma_f32_32x32x16_bf16 v[16:31], v[100:103], v[40:43], v[16:31]\n" \
  "v_mfma_f32_32x32x16_bf16 v[0:15], v[100:103], v[44:47], v[0:15]\n" \
  "ds_read_b128 v[100:103], v114 offset:1536\n" \
  "v_mbcnt_lo_u32_b32 v118, -1, 0\n" \
  "v_mbcnt_hi_u32_b32 v118, -1, v118\n" \
  "v_lshlrev_b32 v119, 4, v118\n" \
  "global_load_dwordx4 v[72:75], v119, %[aub] offset:1792\n" \
  "v_lshlrev_b32 v120, 3, v118\n" \
  "global_load_dwordx2 v[76:77], v120, %[aub] offset:2816\n" \
  "v_mul_u32_u24 v121, 12, v118\n" \
  "global_load_dword v78, v121, %[aub] offset:1032\n" \
  "v_and_b32 v120, 3, v118\n" \
  "v_lshlrev_b32 v120, 4, v120\n" \
  "v_lshrrev_b32 v118, 2, v118\n" \
  "v_lshl_add_u32 v119, v118, 8, v120\n" \
  "global_load_dwordx4 v[64:67], v119, %[xb]\n" \
  "v_min_u32 v118, 8, v118\n" \
  "v_add_u32 v118, 16, v118\n" \
  "v_lshl_add_u32 v119, v118, 8, v120\n" \
  "global_load_dwordx4 v[68:71], v119, %[xb]\n" \
  "s_branch LL2_%=\n" \
  "LP2_%=:\n" \
  "s_waitcnt lgkmcnt(0)\n" \
  "v_mfma_f32_32x32x16_bf16 v[16:31], v[96:99], v[32:35], v[16:31]\n" \
  "v_mfma_f32_32x32x16_bf16 v[0:15], v[96:99], v[36:39], v[0:15]\n" \
  "v_mfma_f32_32x32x16_bf16 v[16:31], v[100:103], v[40:43], v[16:31]\n" \
  "v_mfma_f32_32x32x16_bf16 v[0:15], v[100:103], v[44:47], v[0:15]\n" \
  "ds_read_b128 v[100:103], v114 offset:1664\n" \
  "v_mbcnt_lo_u32_b32 v118, -1, 0\n" \
  "v_mbcnt_hi_u32_b32 v118, -1, v118\n" \
  "v_lshlrev_b32 v119, 4, v118\n" \
  "global_load_dwordx4 v[56:59], v119, %[aub] offset:1792\n" \
  "v_lshlrev_b32 v120, 3, v118\n" \
  "global_load_dwordx2 v[60:61], v120, %[aub] offset:2816\n" \
  "v_mul_u32_u24 v121, 12, v118\n" \
  "global_load_dword v62, v121, %[aub] offset:1032\n" \
  "v_and_b32 v120, 3, v118\n" \
  "v_lshlrev_b32 v120, 4, v120\n" \
  "v_lshrrev_b32 v118, 2, v118\n" \
  "v_lshl_add_u32 v119, v118, 8, v120\n" \
  "global_load_dwordx4 v[48:51], v119, %[xb]\n" \
  "v_min_u32 v118, 8, v118\n" \
  "v_add_u32 v118, 16, v118\n" \
  "v_lshl_add_u32 v119, v118, 8, v120\n" \
  "global_load_dwordx4 v[52:55], v119, %[xb]\n" \
  "s_branch LL3_%=\n" \
  "LP3_%=:\n" \
  "s_waitcnt lgkmcnt(0)\n" \
  "v_mfma_f32_32x32x16_bf16 v[16:31], v[96:99], v[32:35], v[16:31]\n" \
  "v_mfma_f32_32x32x16_bf16 v[0:15], v[96:99], v[36:39], v[0:15]\n" \
  "v_mfma_f32_32x32x16_bf16 v[16:31], v[100:103], v[40:43], v[16:31]\n" \
  "v_mfma_f32_32x32x16_bf16 v[0:15], v[100:103], v[44:47], v[0:15]\n" \
  "ds_read_b128 v[100:103], v114 offset:0\n" \
  "v_mbcnt_lo_u32_b32 v118, -1, 0\n" \
  "v_mbcnt_hi_u32_b32 v118, -1, v118\n" \
  "v_lshlrev_b32 v119, 4, v118\n" \
  "global_load_dwordx4 v[72:75], v119, %[aub] offset:1792\n" \
  "v_lshlrev_b32 v120, 3, v118\n" \
  "global_load_dwordx2 v[76:77], v120, %[aub] offset:2816\n" \
  "v_mul_u32_u24 v121, 12, v118\n" \
  "global_load_dword v78, v121, %[aub] offset:1032\n" \
  "v_and_b32 v120, 3, v118\n" \
  "v_lshlrev_b32 v120, 4, v120\n" \
  "v_lshrrev_b32 v118, 2, v118\n" \
  "v_lshl_add_u32 v119, v118, 8, v120\n" \
  "global_load_dwordx4 v[64:67], v119, %[xb]\n" \
  "v_min_u32 v118, 8, v118\n" \
  "v_add_u32 v118, 16, v118\n" \
  "v_lshl_add_u32 v119, v118, 8, v120\n" \
  "global_load_dwordx4 v[68:71], v119, %[xb]\n" \
  "s_branch LL0_%=\n" \
  "LL0_%=:\n" \
  "s_setprio 0\n" \
  "s_waitcnt vmcnt(5)\n" \
  "v_mfma_f32_32x32x16_bf16 v[32:47], v[88:91], v[80:83], v[48:63]\n" \
  "v_mfma_f32_32x32x16_bf16 v[32:47], v[92:95], v[84:87], v[32:47]\n" \
  "s_waitcnt lgkmcnt(0)\n" \
  "v_lshl_add_u64 v[100:101], v[100:101], 0, v[116:117]\n" \
  "v_lshl_add_u64 v[102:103], v[102:103], 0, v[116:117]\n" \
  "ds_read_b64_tr_b16 v[96:97], v100\n" \
  "ds_read_b64_tr_b16 v[98:99], v101\n" \
  "ds_read_b64_tr_b16 v[100:101], v102\n" \
  "ds_read_b64_tr_b16 v[102:103], v103\n" \
  "s_nop 4\n" \
  "s_setprio 1\n" \
  "v_exp_f32 v32, v32\n" \
  "v_exp_f32 v33, v33\n" \
  "v_exp_f32 v34, v34\n" \
  "v_exp_f32 v35, v35\n" \
  "v_exp_f32 v36, v36\n" \
  "v_exp_f32 v37, v37\n" \
  "v_exp_f32 v38, v38\n" \
  "v_exp_f32 v39, v39\n" \
  "v_exp_f32 v40, v40\n" \
  "v_exp_f32 v41, v41\n" \
  "v_exp_f32 v42, v42\n" \
  "v_exp_f32 v43, v43\n" \
  "v_exp_f32 v44, v44\n" \
  "v_exp_f32 v45, v45\n" \
  "v_exp_f32 v46, v46\n" \
  "v_exp_f32 v47, v47\n" \
  "s_nop 1\n" \
  "v_mfma_f32_32x32x16_bf16 v[32:47], v[122:125], v[122:125], v[32:47]\n" \
  "s_setprio 2\n" \
  "v_lshlrev_b32 v48, 16, v48\n" \
  "v_lshlrev_b32 v49, 16, v49\n" \
  "v_lshlrev_b32 v50, 16, v50\n" \
  "v_lshlrev_b32 v51, 16, v51\n" \
  "v_lshlrev_b32 v52, 16, v52\n" \
  "v_lshlrev_b32 v53, 16, v53\n" \
  "v_lshlrev_b32 v54, 16, v54\n" \
  "v_lshlrev_b32 v55, 16, v55\n" \
  "v_lshlrev_b32 v56, 16, v56\n" \
  "v_lshlrev_b32 v57, 16, v57\n" \
  "v_lshlrev_b32 v58, 16, v58\n" \
  "v_lshlrev_b32 v59, 16, v59\n" \
  "v_lshlrev_b32 v60, 16, v60\n" \
  "v_lshlrev_b32 v61, 16, v61\n" \
  "v_lshlrev_b32 v62, 16, v62\n" \
  "v_lshlrev_b32 v63, 16, v63\n" \
  "v_rcp_f32 v32, v32\n" \
  "v_rcp_f32 v33, v33\n" \
  "v_rcp_f32 v34, v34\n" \
  "v_rcp_f32 v35, v35\n" \
  "v_rcp_f32 v36, v36\n" \
  "v_rcp_f32 v37, v37\n" \
  "v_rcp_f32 v38, v38\n" \
  "v_rcp_f32 v39, v39\n" \
  "v_rcp_f32 v40, v40\n" \
  "v_rcp_f32 v41, v41\n" \
  "v_rcp_f32 v42, v42\n" \
  "v_rcp_f32 v43, v43\n" \
  "v_rcp_f32 v44, v44\n" \
  "v_rcp_f32 v45, v45\n" \
  "v_rcp_f32 v46, v46\n" \
  "v_rcp_f32 v47, v47\n" \
  "s_setprio 3\n" \
  "v_pk_mul_f32 v[48:49], v[32:33], v[48:49]\n" \
  "v_pk_mul_f32 v[50:51], v[34:35], v[50:51]\n" \
  "v_pk_mul_f32 v[52:53], v[36:37], v[52:53]\n" \
  "v_pk_mul_f32 v[54:55], v[38:39], v[54:55]\n" \
  "v_pk_mul_f32 v[56:57], v[40:41], v[56:57]\n" \
  "v_pk_mul_f32 v[58:59], v[42:43], v[58:59]\n" \
  "v_pk_mul_f32 v[60:61], v[44:45], v[60:61]\n" \
  "v_pk_mul_f32 v[62:63], v[46:47], v[62:63]\n" \
  "v_cvt_pk_bf16_f32 v32, v32, v33\n" \
  "v_cvt_pk_bf16_f32 v33, v34, v35\n" \
  "v_cvt_pk_bf16_f32 v34, v36, v37\n" \
  "v_cvt_pk_bf16_f32 v35, v38, v39\n" \
  "v_cvt_pk_bf16_f32 v36, v48, v49\n" \
  "v_cvt_pk_bf16_f32 v37, v50, v51\n" \
  "v_cvt_pk_bf16_f32 v38, v52, v53\n" \
  "v_cvt_pk_bf16_f32 v39, v54, v55\n" \
  "v_cvt_pk_bf16_f32 v40, v40, v41\n" \
  "v_cvt_pk_bf16_f32 v41, v42, v43\n" \
  "v_cvt_pk_bf16_f32 v42, v44, v45\n" \
  "v_cvt_pk_bf16_f32 v43, v46, v47\n" \
  "v_cvt_pk_bf16_f32 v44, v56, v57\n" \
  "v_cvt_pk_bf16_f32 v45, v58, v59\n" \
  "v_cvt_pk_bf16_f32 v46, v60, v61\n" \
  "v_cvt_pk_bf16_f32 v47, v62, v63\n" \
  "s_waitcnt lgkmcnt(0)\n" \
  "v_mfma_f32_32x32x16_bf16 v[16:31], v[96:99], v[32:35], v[16:31]\n" \
  "v_mfma_f32_32x32x16_bf16 v[0:15], v[96:99], v[36:39], v[0:15]\n" \
  "v_mfma_f32_32x32x16_bf16 v[16:31], v[100:103], v[40:43], v[16:31]\n" \
  "v_mfma_f32_32x32x16_bf16 v[0:15], v[100:103], v[44:47], v[0:15]\n" \
  "s_waitcnt vmcnt(0)\n" \
  "s_branch LX_%=\n" \
  "LL1_%=:\n" \
  "s_setprio 0\n" \
  "s_waitcnt vmcnt(5)\n" \
  "v_mfma_f32_32x32x16_bf16 v[32:47], v[88:91], v[80:83], v[64:79]\n" \
  "v_mfma_f32_32x32x16_bf16 v[32:47], v[92:95], v[84:87], v[32:47]\n" \
  "s_waitcnt lgkmcnt(0)\n" \
  "v_lshl_add_u64 v[100:101], v[100:101], 0, v[116:117]\n" \
  "v_lshl_add_u64 v[102:103], v[102:103], 0, v[116:117]\n" \
  "ds_read_b64_tr_b16 v[96:97], v100\n" \
  "ds_read_b64_tr_b16 v[98:99], v101\n" \
  "ds_read_b64_tr_b16 v[100:101], v102\n" \
  "ds_read_b64_tr_b16 v[102:103], v103\n" \
  "s_nop 4\n" \
  "s_setprio 1\n" \
  "v_exp_f32 v32, v32\n" \
  "v_exp_f32 v33, v33\n" \
  "v_exp_f32 v34, v34\n" \
  "v_exp_f32 v35, v35\n" \
  "v_exp_f32 v36, v36\n" \
  "v_exp_f32 v37, v37\n" \
  "v_exp_f32 v38, v38\n" \
  "v_exp_f32 v39, v39\n" \
  "v_exp_f32 v40, v40\n" \
  "v_exp_f32 v41, v41\n" \
  "v_exp_f32 v42, v42\n" \
  "v_exp_f32 v43, v43\n" \
  "v_exp_f32 v44, v44\n" \
  "v_exp_f32 v45, v45\n" \
  "v_exp_f32 v46, v46\n" \
  "v_exp_f32 v47, v47\n" \
  "s_nop 1\n" \
  "v_mfma_f32_32x32x16_bf16 v[32:47], v[122:125], v[122:125], v[32:47]\n" \
  "s_setprio 2\n" \
  "v_lshlrev_b32 v64, 16, v64\n" \
  "v_lshlrev_b32 v65, 16, v65\n" \
  "v_lshlrev_b32 v66, 16, v66\n" \
  "v_lshlrev_b32 v67, 16, v67\n" \
  "v_lshlrev_b32 v68, 16, v68\n" \
  "v_lshlrev_b32 v69, 16, v69\n" \
  "v_lshlrev_b32 v70, 16, v70\n" \
  "v_lshlrev_b32 v71, 16, v71\n" \
  "v_lshlrev_b32 v72, 16, v72\n" \
  "v_lshlrev_b32 v73, 16, v73\n" \
  "v_lshlrev_b32 v74, 16, v74\n" \
  "v_lshlrev_b32 v75, 16, v75\n" \
  "v_lshlrev_b32 v76, 16, v76\n" \
  "v_lshlrev_b32 v77, 16, v77\n" \
  "v_lshlrev_b32 v78, 16, v78\n" \
  "v_lshlrev_b32 v79, 16, v79\n" \
  "v_rcp_f32 v32, v32\n" \
  "v_rcp_f32 v33, v33\n" \
  "v_rcp_f32 v34, v34\n" \
  "v_rcp_f32 v35, v35\n" \
  "v_rcp_f32 v36, v36\n" \
  "v_rcp_f32 v37, v37\n" \
  "v_rcp_f32 v38, v38\n" \
  "v_rcp_f32 v39, v39\n" \
  "v_rcp_f32 v40, v40\n" \
  "v_rcp_f32 v41, v41\n" \
  "v_rcp_f32 v42, v42\n" \
  "v_rcp_f32 v43, v43\n" \
  "v_rcp_f32 v44, v44\n" \
  "v_rcp_f32 v45, v45\n" \
  "v_rcp_f32 v46, v46\n" \
  "v_rcp_f32 v47, v47\n" \
  "s_setprio 3\n" \
  "v_pk_mul_f32 v[64:65], v[32:33], v[64:65]\n" \
  "v_pk_mul_f32 v[66:67], v[34:35], v[66:67]\n" \
  "v_pk_mul_f32 v[68:69], v[36:37], v[68:69]\n" \
  "v_pk_mul_f32 v[70:71], v[38:39], v[70:71]\n" \
  "v_pk_mul_f32 v[72:73], v[40:41], v[72:73]\n" \
  "v_pk_mul_f32 v[74:75], v[42:43], v[74:75]\n" \
  "v_pk_mul_f32 v[76:77], v[44:45], v[76:77]\n" \
  "v_pk_mul_f32 v[78:79], v[46:47], v[78:79]\n" \
  "v_cvt_pk_bf16_f32 v32, v32, v33\n" \
  "v_cvt_pk_bf16_f32 v33, v34, v35\n" \
  "v_cvt_pk_bf16_f32 v34, v36, v37\n" \
  "v_cvt_pk_bf16_f32 v35, v38, v39\n" \
  "v_cvt_pk_bf16_f32 v36, v64, v65\n" \
  "v_cvt_pk_bf16_f32 v37, v66, v67\n" \
  "v_cvt_pk_bf16_f32 v38, v68, v69\n" \
  "v_cvt_pk_bf16_f32 v39, v70, v71\n" \
  "v_cvt_pk_bf16_f32 v40, v40, v41\n" \
  "v_cvt_pk_bf16_f32 v41, v42, v43\n" \
  "v_cvt_pk_bf16_f32 v42, v44, v45\n" \
  "v_cvt_pk_bf16_f32 v43, v46, v47\n" \
  "v_cvt_pk_bf16_f32 v44, v72, v73\n" \
  "v_cvt_pk_bf16_f32 v45, v74, v75\n" \
  "v_cvt_pk_bf16_f32 v46, v76, v77\n" \
  "v_cvt_pk_bf16_f32 v47, v78, v79\n" \
  "s_waitcnt lgkmcnt(0)\n" \
  "v_mfma_f32_32x32x16_bf16 v[16:31], v[96:99], v[32:35], v[16:31]\n" \
  "v_mfma_f32_32x32x16_bf16 v[0:15], v[96:99], v[36:39], v[0:15]\n" \
  "v_mfma_f32_32x32x16_bf16 v[16:31], v[100:103], v[40:43], v[16:31]\n" \
  "v_mfma_f32_32x32x16_bf16 v[0:15], v[100:103], v[44:47], v[0:15]\n" \
  "s_waitcnt vmcnt(0)\n" \
  "v_mov_b32 v64, v48\n" \
  "v_mov_b32 v65, v49\n" \
  "v_mov_b32 v66, v50\n" \
  "v_mov_b32 v67, v51\n" \
  "v_mov_b32 v68, v52\n" \
  "v_mov_b32 v69, v53\n" \
  "v_mov_b32 v70, v54\n" \
  "v_mov_b32 v71, v55\n" \
  "v_mov_b32 v72, v56\n" \
  "v_mov_b32 v73, v57\n" \
  "v_mov_b32 v74, v58\n" \
  "v_mov_b32 v75, v59\n" \
  "v_mov_b32 v76, v60\n" \
  "v_mov_b32 v77, v61\n" \
  "v_mov_b32 v78, v62\n" \
  "s_branch LX_%=\n" \
  "LL2_%=:\n" \
  "s_setprio 0\n" \
  "s_waitcnt vmcnt(5)\n" \
  "v_mfma_f32_32x32x16_bf16 v[32:47], v[88:91], v[80:83], v[48:63]\n" \
  "v_mfma_f32_32x32x16_bf16 v[32:47], v[92:95], v[84:87], v[32:47]\n" \
  "s_waitcnt lgkmcnt(0)\n" \
  "v_lshl_add_u64 v[100:101], v[100:101], 0, v[116:117]\n" \
  "v_lshl_add_u64 v[102:103], v[102:103], 0, v[116:117]\n" \
  "ds_read_b64_tr_b16 v[96:97], v100\n" \
  "ds_read_b64_tr_b16 v[98:99], v101\n" \
  "ds_read_b64_tr_b16 v[100:101], v102\n" \
  "ds_read_b64_tr_b16 v[102:103], v103\n" \
  "s_nop 4\n" \
  "s_setprio 1\n" \
  "v_exp_f32 v32, v32\n" \
  "v_exp_f32 v33, v33\n" \
  "v_exp_f32 v34, v34\n" \
  "v_exp_f32 v35, v35\n" \
  "v_exp_f32 v36, v36\n" \
  "v_exp_f32 v37, v37\n" \
  "v_exp_f32 v38, v38\n" \
  "v_exp_f32 v39, v39\n" \
  "v_exp_f32 v40, v40\n" \
  "v_exp_f32 v41, v41\n" \
  "v_exp_f32 v42, v42\n" \
  "v_exp_f32 v43, v43\n" \
  "v_exp_f32 v44, v44\n" \
  "v_exp_f32 v45, v45\n" \
  "v_exp_f32 v46, v46\n" \
  "v_exp_f32 v47, v47\n" \
  "s_nop 1\n" \
  "v_mfma_f32_32x32x16_bf16 v[32:47], v[122:125], v[122:125], v[32:47]\n" \
  "s_setprio 2\n" \
  "v_lshlrev_b32 v48, 16, v48\n" \
  "v_lshlrev_b32 v49, 16, v49\n" \
  "v_lshlrev_b32 v50, 16, v50\n" \
  "v_lshlrev_b32 v51, 16, v51\n" \
  "v_lshlrev_b32 v52, 16, v52\n" \
  "v_lshlrev_b32 v53, 16, v53\n" \
  "v_lshlrev_b32 v54, 16, v54\n" \
  "v_lshlrev_b32 v55, 16, v55\n" \
  "v_lshlrev_b32 v56, 16, v56\n" \
  "v_lshlrev_b32 v57, 16, v57\n" \
  "v_lshlrev_b32 v58, 16, v58\n" \
  "v_lshlrev_b32 v59, 16, v59\n" \
  "v_lshlrev_b32 v60, 16, v60\n" \
  "v_lshlrev_b32 v61, 16, v61\n" \
  "v_lshlrev_b32 v62, 16, v62\n" \
  "v_lshlrev_b32 v63, 16, v63\n" \
  "v_rcp_f32 v32, v32\n" \
  "v_rcp_f32 v33, v33\n" \
  "v_rcp_f32 v34, v34\n" \
  "v_rcp_f32 v35, v35\n" \
  "v_rcp_f32 v36, v36\n" \
  "v_rcp_f32 v37, v37\n" \
  "v_rcp_f32 v38, v38\n" \
  "v_rcp_f32 v39, v39\n" \
  "v_rcp_f32 v40, v40\n" \
  "v_rcp_f32 v41, v41\n" \
  "v_rcp_f32 v42, v42\n" \
  "v_rcp_f32 v43, v43\n" \
  "v_rcp_f32 v44, v44\n" \
  "v_rcp_f32 v45, v45\n" \
  "v_rcp_f32 v46, v46\n" \
  "v_rcp_f32 v47, v47\n" \
  "s_setprio 3\n" \
  "v_pk_mul_f32 v[48:49], v[32:33], v[48:49]\n" \
  "v_pk_mul_f32 v[50:51], v[34:35], v[50:51]\n" \
  "v_pk_mul_f32 v[52:53], v[36:37], v[52:53]\n" \
  "v_pk_mul_f32 v[54:55], v[38:39], v[54:55]\n" \
  "v_pk_mul_f32 v[56:57], v[40:41], v[56:57]\n" \
  "v_pk_mul_f32 v[58:59], v[42:43], v[58:59]\n" \
  "v_pk_mul_f32 v[60:61], v[44:45], v[60:61]\n" \
  "v_pk_mul_f32 v[62:63], v[46:47], v[62:63]\n" \
  "v_cvt_pk_bf16_f32 v32, v32, v33\n" \
  "v_cvt_pk_bf16_f32 v33, v34, v35\n" \
  "v_cvt_pk_bf16_f32 v34, v36, v37\n" \
  "v_cvt_pk_bf16_f32 v35, v38, v39\n" \
  "v_cvt_pk_bf16_f32 v36, v48, v49\n" \
  "v_cvt_pk_bf16_f32 v37, v50, v51\n" \
  "v_cvt_pk_bf16_f32 v38, v52, v53\n" \
  "v_cvt_pk_bf16_f32 v39, v54, v55\n" \
  "v_cvt_pk_bf16_f32 v40, v40, v41\n" \
  "v_cvt_pk_bf16_f32 v41, v42, v43\n" \
  "v_cvt_pk_bf16_f32 v42, v44, v45\n" \
  "v_cvt_pk_bf16_f32 v43, v46, v47\n" \
  "v_cvt_pk_bf16_f32 v44, v56, v57\n" \
  "v_cvt_pk_bf16_f32 v45, v58, v59\n" \
  "v_cvt_pk_bf16_f32 v46, v60, v61\n" \
  "v_cvt_pk_bf16_f32 v47, v62, v63\n" \
  "s_waitcnt lgkmcnt(0)\n" \
  "v_mfma_f32_32x32x16_bf16 v[16:31], v[96:99], v[32:35], v[16:31]\n" \
  "v_mfma_f32_32x32x16_bf16 v[0:15], v[96:99], v[36:39], v[0:15]\n" \
  "v_mfma_f32_32x32x16_bf16 v[16:31], v[100:103], v[40:43], v[16:31]\n" \
  "v_mfma_f32_32x32x16_bf16 v[0:15], v[100:103], v[44:47], v[0:15]\n" \
  "s_waitcnt vmcnt(0)\n" \
  "s_branch LX_%=\n" \
  "LL3_%=:\n" \
  "s_setprio 0\n" \
  "s_waitcnt vmcnt(5)\n" \
  "v_mfma_f32_32x32x16_bf16 v[32:47], v[88:91], v[80:83], v[64:79]\n" \
  "v_mfma_f32_32x32x16_bf16 v[32:47], v[92:95], v[84:87], v[32:47]\n" \
  "s_waitcnt lgkmcnt(0)\n" \
  "v_lshl_add_u64 v[100:101], v[100:101], 0, v[116:117]\n" \
  "v_lshl_add_u64 v[102:103], v[102:103], 0, v[116:117]\n" \
  "ds_read_b64_tr_b16 v[96:97], v100\n" \
  "ds_read_b64_tr_b16 v[98:99], v101\n" \
  "ds_read_b64_tr_b16 v[100:101], v102\n" \
  "ds_read_b64_tr_b16 v[102:103], v103\n" \
  "s_nop 4\n" \
  "s_setprio 1\n" \
  "v_exp_f32 v32, v32\n" \
  "v_exp_f32 v33, v33\n" \
  "v_exp_f32 v34, v34\n" \
  "v_exp_f32 v35, v35\n" \
  "v_exp_f32 v36, v36\n" \
  "v_exp_f32 v37, v37\n" \
  "v_exp_f32 v38, v38\n" \
  "v_exp_f32 v39, v39\n" \
  "v_exp_f32 v40, v40\n" \
  "v_exp_f32 v41, v41\n" \
  "v_exp_f32 v42, v42\n" \
  "v_exp_f32 v43, v43\n" \
  "v_exp_f32 v44, v44\n" \
  "v_exp_f32 v45, v45\n" \
  "v_exp_f32 v46, v46\n" \
  "v_exp_f32 v47, v47\n" \
  "s_nop 1\n" \
  "v_mfma_f32_32x32x16_bf16 v[32:47], v[122:125], v[122:125], v[32:47]\n" \
  "s_setprio 2\n" \
  "v_lshlrev_b32 v64, 16, v64\n" \
  "v_lshlrev_b32 v65, 16, v65\n" \
  "v_lshlrev_b32 v66, 16, v66\n" \
  "v_lshlrev_b32 v67, 16, v67\n" \
  "v_lshlrev_b32 v68, 16, v68\n" \
  "v_lshlrev_b32 v69, 16, v69\n" \
  "v_lshlrev_b32 v70, 16, v70\n" \
  "v_lshlrev_b32 v71, 16, v71\n" \
  "v_lshlrev_b32 v72, 16, v72\n" \
  "v_lshlrev_b32 v73, 16, v73\n" \
  "v_lshlrev_b32 v74, 16, v74\n" \
  "v_lshlrev_b32 v75, 16, v75\n" \
  "v_lshlrev_b32 v76, 16, v76\n" \
  "v_lshlrev_b32 v77, 16, v77\n" \
  "v_lshlrev_b32 v78, 16, v78\n" \
  "v_lshlrev_b32 v79, 16, v79\n" \
  "v_rcp_f32 v32, v32\n" \
  "v_rcp_f32 v33, v33\n" \
  "v_rcp_f32 v34, v34\n" \
  "v_rcp_f32 v35, v35\n" \
  "v_rcp_f32 v36, v36\n" \
  "v_rcp_f32 v37, v37\n" \
  "v_rcp_f32 v38, v38\n" \
  "v_rcp_f32 v39, v39\n" \
  "v_rcp_f32 v40, v40\n" \
  "v_rcp_f32 v41, v41\n" \
  "v_rcp_f32 v42, v42\n" \
  "v_rcp_f32 v43, v43\n" \
  "v_rcp_f32 v44, v44\n" \
  "v_rcp_f32 v45, v45\n" \
  "v_rcp_f32 v46, v46\n" \
  "v_rcp_f32 v47, v47\n" \
  "s_setprio 3\n" \
  "v_pk_mul_f32 v[64:65], v[32:33], v[64:65]\n" \
  "v_pk_mul_f32 v[66:67], v[34:35], v[66:67]\n" \
  "v_pk_mul_f32 v[68:69], v[36:37], v[68:69]\n" \
  "v_pk_mul_f32 v[70:71], v[38:39], v[70:71]\n" \
  "v_pk_mul_f32 v[72:73], v[40:41], v[72:73]\n" \
  "v_pk_mul_f32 v[74:75], v[42:43], v[74:75]\n" \
  "v_pk_mul_f32 v[76:77], v[44:45], v[76:77]\n" \
  "v_pk_mul_f32 v[78:79], v[46:47], v[78:79]\n" \
  "v_cvt_pk_bf16_f32 v32, v32, v33\n" \
  "v_cvt_pk_bf16_f32 v33, v34, v35\n" \
  "v_cvt_pk_bf16_f32 v34, v36, v37\n" \
  "v_cvt_pk_bf16_f32 v35, v38, v39\n" \
  "v_cvt_pk_bf16_f32 v36, v64, v65\n" \
  "v_cvt_pk_bf16_f32 v37, v66, v67\n" \
  "v_cvt_pk_bf16_f32 v38, v68, v69\n" \
  "v_cvt_pk_bf16_f32 v39, v70, v71\n" \
  "v_cvt_pk_bf16_f32 v40, v40, v41\n" \
  "v_cvt_pk_bf16_f32 v41, v42, v43\n" \
  "v_cvt_pk_bf16_f32 v42, v44, v45\n" \
  "v_cvt_pk_bf16_f32 v43, v46, v47\n" \
  "v_cvt_pk_bf16_f32 v44, v72, v73\n" \
  "v_cvt_pk_bf16_f32 v45, v74, v75\n" \
  "v_cvt_pk_bf16_f32 v46, v76, v77\n" \
  "v_cvt_pk_bf16_f32 v47, v78, v79\n" \
  "s_waitcnt lgkmcnt(0)\n" \
  "v_mfma_f32_32x32x16_bf16 v[16:31], v[96:99], v[32:35], v[16:31]\n" \
  "v_mfma_f32_32x32x16_bf16 v[0:15], v[96:99], v[36:39], v[0:15]\n" \
  "v_mfma_f32_32x32x16_bf16 v[16:31], v[100:103], v[40:43], v[16:31]\n" \
  "v_mfma_f32_32x32x16_bf16 v[0:15], v[100:103], v[44:47], v[0:15]\n" \
  "s_waitcnt vmcnt(0)\n" \
  "v_mov_b32 v64, v48\n" \
  "v_mov_b32 v65, v49\n" \
  "v_mov_b32 v66, v50\n" \
  "v_mov_b32 v67, v51\n" \
  "v_mov_b32 v68, v52\n" \
  "v_mov_b32 v69, v53\n" \
  "v_mov_b32 v70, v54\n" \
  "v_mov_b32 v71, v55\n" \
  "v_mov_b32 v72, v56\n" \
  "v_mov_b32 v73, v57\n" \
  "v_mov_b32 v74, v58\n" \
  "v_mov_b32 v75, v59\n" \
  "v_mov_b32 v76, v60\n" \
  "v_mov_b32 v77, v61\n" \
  "v_mov_b32 v78, v62\n" \
  "LX_%=:\n" \
  "s_nop 15\n"

__global__ __launch_bounds__(256, 4) void gg_agg_kernel(
    const u16* __restrict__ x, const u16* __restrict__ w_c, char* __restrict__ ws, u16* __restrict__ out) {
  __shared__ __attribute__((aligned(16))) char smem[L_TOTAL];
  const int tid = threadIdx.x;
  const int lane = tid & 63;
  const int w = __builtin_amdgcn_readfirstlane(tid >> 6);
  const int b = blockIdx.x;
  const int g = (b & 7) >> 1;
  const int fb = (((b & 1) * 125 + (b >> 3)) << 2) + w;
  const int m = lane & 31, h = lane >> 5;
  const int G = lane >> 4, q4 = (lane & 15) >> 2, p4 = lane & 3;
  const u32 sb = lds_addr_of(smem);
  __builtin_amdgcn_s_setprio(3);

  const u32* offs = (const u32*)(ws + WS_OFFS) + fb;
  u32 oe = offs[min(lane, NCHUNK - 1) * 1024];
  const char* au = ws + WS_AU + (size_t)(fb * 4 + g) * AU_UNIT;
  u32x4 nb0 = *(const u32x4*)(au + lane * 16);
  u32x3 nbq = *(const u32x3*)(au + 1024 + lane * 12);
  u32 wc0 = w_c[(h ? 0 : 2) * DIM + g * 32 + m];
  u32 wc1 = w_c[(h ? 1 : 3) * DIM + g * 32 + m];

  {
    const u32 lsw = sb + L_LS + w * 1792, law = sb + L_LA + w * 512;
    lds_st<u32x4>(lsw + lane * 16, u32x4{0u, 0u, 0u, 0u});
    lds_st<u32x4>(lsw + 768 + lane * 16, u32x4{0u, 0u, 0u, 0u});
    lds_st<u32x2>(law + lane * 8, u32x2{0u, 0u});
    const u32 r = lane >> 2, j = lane & 7;
    const u32 row = 16 * (w & 1) + 4 * (w >> 1) + (j & 3) + ((j & 4) << 1);
    const u32 aS = lsw + (7 * w - 1) * 2 + r * 258 + (lane & 3) * 64;
    const u32 aA = law + 16 + row * 16 + j * 2;
    const bool isA = lane >= 32;
    const u32 key = isA ? row : 7 * w - 1 + r;
    const u32 lim = isA ? 40 : 28;
    if ((u32)lane < lim && key < (u32)NPB) lds_st<u16>(isA ? aA : aS, (u16)0x3f80);
  }
  const u32 wb = sb + w * WAVE_LDS;
  const u32 ex = wb + 2 * SLOT;
  const u32x4 rrs = {(u32)(size_t)(ws + WS_RECS), (u32)((size_t)(ws + WS_RECS) >> 32) & 0xffffu, (u32)(NCHUNK * CH * 12), 0x00020000u};
  const size_t tb = (size_t)(ws + WS_TABLE + (size_t)g * N_NODES * 128 - 128);
  const u32x4 trs = {(u32)tb, ((u32)(tb >> 32) & 0xffffu) | (4u << 16), 0x7fffffffu, 1u << 23};
  const __amdgpu_buffer_rsrc_t rrs_c = __builtin_amdgcn_make_buffer_rsrc((void*)(ws + WS_RECS), 0, NCHUNK * CH * 12, 0x00020000);

  u32x8 nb;
  {
    float a = __builtin_bit_cast(float, wc0 << 16) * NLOG2E;
    float c = __builtin_bit_cast(float, wc1 << 16) * NLOG2E;
    nb = u32x8{nb0[0], nb0[1], nb0[2], nb0[3], nbq[0], nbq[1], nbq[2] & 0xffffu, cvtpk(a, c)};
  }

  f32x16 num, den;
  asm volatile("" : "={v[0:15]}"(num), "={v[16:31]}"(den));

  const u32 b_so = wb + lane * 4;
  const u32 b_ds = wb + 256 + ((lane & 32) + ((lane & 7) << 2) + ((lane >> 3) & 3)) * 4;
  const u32 hadj = (lane & 4) ? 0u : 128u;
  const u32 lsb = sb + L_LS + (lane & 3) * 64;
  const u32 lab = sb + L_LA, lab1 = lab + 1024;
  const u32 c_so = wb + h * 16;
  const u32 c_ds = wb + 256 + (h * 4 + q4) * 16;
  const u32 c_ae = wb + 512 + h * 512 + m * 4;
  const u32 c_ls = (G & 1) * 32 + p4 * 8;
  const u32x2 c_ls2 = {c_ls, c_ls};
  const u32x4 ones = {0x3e803e80u, 0x3e803e80u, 0x3e803e80u, 0x3e803e80u};
  const char* xb = (const char*)x + (size_t)fb * (NPB * 256) + g * 64;

  u32x16 ep;
  int M;
  auto window = [&](u32 oe_, int w0, bool first) __attribute__((always_inline)) {
    int ln = lane;
    asm volatile("" : "+v"(ln));
    oe_ = (ln < NCHUNK) ? oe_ : 0u;
    const u32 cnt = oe_ >> 16;
    const u32 inc = wave_incl_scan(cnt);
    const u32 pre = inc - cnt;
    M = __builtin_amdgcn_readlane(inc, 63);
    const u32 rbase = (ln * CH + (oe_ & 0xffffu)) * 12u;
    const int mw = min(WIN, M - w0);
    {
      u32 mk;
      asm volatile("v_mov_b32 %0, 0x7ffffff0" : "=v"(mk));
      lds_st<u32x4>(ex + ln * 16, u32x4{mk, mk, mk, mk});
      lds_st<u32x4>(ex + 1024 + ln * 16, u32x4{mk, mk, mk, mk});
      lds_st<u32x2>(ex + 2048 + ln * 8, u32x2{mk, mk});
      int jlo = max(0, w0 - (int)pre);
      int jhi = min((int)cnt, w0 + WIN - (int)pre);
      u32 la = ex + ((int)pre + jlo - w0) * 4;
      u32 lend = ex + ((int)pre + jhi - w0) * 4;
      u32 rv = rbase + jlo * 12;
      u32x2 rp = {rv, rv + 12};
      u32 odd = (jhi - jlo) & (jhi > jlo ? 1 : 0);
      unsigned long long sv;
      asm volatile(
          "v_cmp_ne_u32 vcc, 0, %[odd]\n"
          "s_and_saveexec_b64 %[sv], vcc\n"
          "ds_write_b32 %[la], v40\n"
          "v_add_u32 %[la], 4, %[la]\n"
          "v_lshl_add_u64 v[40:41], v[40:41], 0, %[c12]\n"
          "s_mov_b64 exec, %[sv]\n"
          "v_cmp_lt_i32 vcc, %[la], %[lend]\n"
          "s_and_b64 exec, exec, vcc\n"
          "s_cbranch_execz EXD_%=\n"
          "EXL_%=:\n"
          "ds_write2_b32 %[la], v40, v41 offset1:1\n"
          "v_add_u32 %[la], 8, %[la]\n"
          "v_lshl_add_u64 v[40:41], v[40:41], 0, %[c24]\n"
          "v_cmp_lt_i32 vcc, %[la], %[lend]\n"
          "s_and_b64 exec, exec, vcc\n"
          "s_cbranch_execnz EXL_%=\n"
          "EXD_%=:\n"
          "s_mov_b64 exec, %[sv]\n"
          : [la] "+v"(la), "+{v[40:41]}"(rp), [sv] "=&s"(sv)
          : [odd] "v"(odd), [lend] "v"(lend), [c12] "s"(0x0000000c0000000cull), [c24] "s"(0x0000001800000018ull)
          : "vcc", "memory");
    }
    u32 exl = ex + ln * 4;
    u32x3 rec0 = __builtin_bit_cast(u32x3, __builtin_amdgcn_raw_buffer_load_b96(rrs_c, lds_ld<u32>(exl), 0, 0));
    u32x3 rec = __builtin_bit_cast(u32x3, __builtin_amdgcn_raw_buffer_load_b96(rrs_c, lds_ld<u32>(exl + 256), 0, 0));
    exl += 512;
    if (first) {
      __syncthreads();
    }
    {
      u32 r0 = rec0[0];
      u32 f = r0 & 31u;
      lds_st<u32>(b_so, (r0 & ~127u) + hadj);
      lds_st<u32>(b_ds, f * 256 + lsb);
      lds_st<u32>(b_so + 512, f * 16 + lab);
      lds_st<u32>(b_so + 768, rec0[2]);
      lds_st<u32>(b_so + 1024, f * 16 + lab1);
      lds_st<u32>(b_so + 1280, rec0[1]);
    }
    int rem = max(1, (mw + 31) >> 5);
    asm volatile(K2_MAIN_ASM
        : "+{v[0:15]}"(num), "+{v[16:31]}"(den), "={v[64:79]}"(ep), "+{v[104:106]}"(rec), "+{v108}"(exl), [rem] "+s"(rem)
        : "{v[80:87]}"(nb), "{v109}"(b_so), "{v110}"(b_ds), "{v111}"(hadj), "{v112}"(lsb), "{v113}"(c_so), "{v114}"(c_ds),
          "{v115}"(c_ae), "{v[116:117]}"(c_ls2), "{v[122:125]}"(ones), [first] "s"(first ? 1 : 0), [trs] "s"(trs), [rrs] "s"(rrs), [lab] "s"(lab), [lab1] "s"(lab1), [xb] "s"(xb), [aub] "s"(au)
        : "memory", "scc",
          "v32", "v33", "v34", "v35", "v36", "v37", "v38", "v39", "v40", "v41", "v42", "v43", "v44", "v45", "v46", "v47",
          "v48", "v49", "v50", "v51", "v52", "v53", "v54", "v55", "v56", "v57", "v58", "v59", "v60", "v61", "v62", "v63",
          "v88", "v89", "v90", "v91", "v92", "v93", "v94", "v95", "v96", "v97", "v98", "v99", "v100", "v101", "v102", "v103",
          "v107", "v118", "v119", "v120", "v121");
  };
  window(oe, 0, true);
  for (int w0 = WIN; w0 < M; w0 += WIN) {
    int ln = lane;
    asm volatile("" : "+v"(ln));
    window(offs[min(ln, NCHUNK - 1) * 1024], w0, false);
  }

  {
    int ln = lane;
    asm volatile("" : "+v"(ln));
    const int mm = ln & 31, hh = ln >> 5;
    const u32 epa = wb + hh * 512 + mm * 4;
#pragma unroll
    for (int i = 0; i < 13; ++i) {
      u32 up = (i < 8) ? ep[8 + (i >> 1)] : (i < 12) ? ep[12 + ((i - 8) >> 1)] : ep[14];
      float u = __builtin_bit_cast(float, (i & 1) || i == 12 ? (up & 0xffff0000u) : (up << 16));
      float z = u + num[i] * __builtin_amdgcn_rcpf(den[i] + 1e-6f);
      if (i < 12 || hh == 0) lds_st<float>(epa + ((i & 3) + 8 * (i >> 2)) * 128, fmaxf(z, 0.f));
    }
    const u32 rp = wb + ln * 32;
    const u32 xo = (u32)(fb * NPB + (ln >> 2)) * 256 + g * 64 + (ln & 3) * 16;
    const rsrc_t outr = mk_rsrc(out, N_NODES * DIM * 2);
    auto fin = [&](u32x4 xv, u32 a, u32 o) {
      f32x4 r0 = lds_ld<f32x4>(a), r1 = lds_ld<f32x4>(a + 16);
      u32x4 ov;
#pragma unroll
      for (int k = 0; k < 2; ++k) {
        ov[k] = cvtpk(__builtin_bit_cast(float, xv[k] << 16) + r0[2 * k], __builtin_bit_cast(float, xv[k] & 0xffff0000u) + r0[2 * k + 1]);
        ov[2 + k] = cvtpk(__builtin_bit_cast(float, xv[2 + k] << 16) + r1[2 * k], __builtin_bit_cast(float, xv[2 + k] & 0xffff0000u) + r1[2 * k + 1]);
      }
      bst<OUTAUX>(outr, o, ov);
    };
    fin(u32x4{ep[0], ep[1], ep[2], ep[3]}, rp, xo);
    if (ln < 36) fin(u32x4{ep[4], ep[5], ep[6], ep[7]}, rp + 2048, xo + 16 * 256);
  }
}

extern "C" void kernel_launch(void* const* d_in, const int* in_sizes, int n_in,
                              void* d_out, int out_size, void* d_ws, size_t ws_size,
                              hipStream_t stream) {
  const u16* x = (const u16*)d_in[0];
  const u32* eattr = (const u32*)d_in[1];
  const int* src = (const int*)d_in[2];
  const int* dst = (const int*)d_in[3];
  const u16* w_a = (const u16*)d_in[4];
  const u16* w_b = (const u16*)d_in[5];
  const u16* w_c = (const u16*)d_in[6];
  const u16* w_u = (const u16*)d_in[7];
  const u16* w_v = (const u16*)d_in[8];
  char* ws = (char*)d_ws;
  gg_prep_kernel<<<NCHUNK + GEMM_BLOCKS, K1_THREADS, 0, stream>>>(x, eattr, src, dst, w_a, w_b, w_u, w_v, ws);
  gg_agg_kernel<<<NBUCK, 256, 0, stream>>>(x, w_c, ws, (u16*)d_out);
}
```
